```python
import torch

def _softmax(x, axis=-1):
    return torch.softmax(x, dim=axis)
def _gelu(x):
    return torch.nn.functional.gelu(x, approximate="tanh")
def _silu(x):
    return torch.nn.functional.silu(x)
def _sigmoid(x):
    return torch.sigmoid(x)
def _layernorm(x, axis=-1, eps=1e-5):
    m = x.mean(dim=axis, keepdim=True)
    v = x.var(dim=axis, keepdim=True, unbiased=False)
    return (x - m) / torch.sqrt(v + eps)
def _scatter_add(tgt, idx, src, axis=0):
    return tgt.clone().index_add_(axis, idx, src)
def _segment_sum(src, seg, n):
    out = torch.zeros((n,) + src.shape[1:], dtype=src.dtype)
    return out.index_add_(0, seg, src)
def _pad(x, pw):
    flat = tuple(v for pair in reversed(pw) for v in pair)
    return torch.nn.functional.pad(x, flat)

kernel_name = 'interleave_layers__linear_kernel_hedgehog__glu_gated_geglu__prompt'

def setup_inputs():

    _w = (lambda a: (lambda t: t.to(torch.bfloat16) if t.is_floating_point() else t)(torch.as_tensor(a)))
    def _A_setup():
        B, S, D, H = 1, 2048, 640, 32
        Dh = D // H
        return {
            "x": _w((lambda *s: torch.randn(*s, dtype=torch.bfloat16) * 0.05)(B, S, D)),
            "wqkv": _w((lambda *s: torch.randn(*s, dtype=torch.bfloat16) * 0.4)(D, 3 * D)),
            "w_phi": _w((lambda *s: torch.randn(*s, dtype=torch.bfloat16) * 0.4)(H, Dh, Dh) * (Dh ** -0.5)),
            "wo": _w((lambda *s: torch.randn(*s, dtype=torch.bfloat16) * 0.05)(D, D)),
        }
    _A_in = _A_setup()

    def _B_setup():
        B, S, Dm, Df = 1, 2048, 640, 1728
        return {
            "x": _w((lambda *s: torch.randn(*s, dtype=torch.bfloat16) * 0.05)(B, S, Dm)),
            "w_gate": _w((lambda *s: torch.randn(*s, dtype=torch.bfloat16) * 1.6)(Dm, Df) * (Dm ** -0.5)),
            "w_up": _w((lambda *s: torch.randn(*s, dtype=torch.bfloat16) * 1.6)(Dm, Df) * (Dm ** -0.5)),
            "w_down": _w((lambda *s: torch.randn(*s, dtype=torch.bfloat16) * 1.6)(Df, Dm) * (Df ** -0.5)),
        }
    _B_in = _B_setup()

    return {
        "A_x": _A_in["x"],
        "A_wqkv": _A_in["wqkv"],
        "A_w_phi": _A_in["w_phi"],
        "A_wo": _A_in["wo"],
        "B_x": _B_in["x"],
        "B_w_gate": _B_in["w_gate"],
        "B_w_up": _B_in["w_up"],
        "B_w_down": _B_in["w_down"],
    }

def reference(A_x, A_wqkv, A_w_phi, A_wo, B_x, B_w_gate, B_w_up, B_w_down):
    def _A_ref(x, wqkv, w_phi, wo):
        B, S, D = x.shape
        H = 32; Dh = D // H
        qkv = (lambda x, *d: x.permute(*d))((x @ wqkv).reshape(B, S, 3, H, Dh), 2, 0, 3, 1, 4)
        q, k, v = qkv[0], qkv[1], qkv[2]
        pq = torch.einsum("bhsd,hdf->bhsf", q, w_phi)
        pk = torch.einsum("bhsd,hdf->bhsf", k, w_phi)
        phi_q = torch.concatenate([_softmax(pq, axis=-1), _softmax(-pq, axis=-1)], axis=-1)
        phi_k = torch.concatenate([_softmax(pk, axis=-1), _softmax(-pk, axis=-1)], axis=-1)
        kv = torch.einsum("bhsf,bhsd->bhfd", phi_k, v)
        z  = torch.einsum("bhsf->bhf", phi_k)
        num = torch.einsum("bhqf,bhfd->bhqd", phi_q, kv)
        den = torch.einsum("bhqf,bhf->bhq", phi_q, z)[..., None] + 1e-6
        o = num / den
        return (lambda x, *d: x.permute(*d))(o, 0, 2, 1, 3).reshape(B, S, D) @ wo

    def _B_ref(x, w_gate, w_up, w_down):
        g = torch.einsum("bsd,df->bsf", x, w_gate)
        u = torch.einsum("bsd,df->bsf", x, w_up)
        h = _gelu(g) * u
        return torch.einsum("bsf,fd->bsd", h, w_down)

    x = A_x
    x = x + _A_ref(_layernorm(x, axis=-1), A_wqkv, A_w_phi, A_wo)
    x = x + _B_ref(_layernorm(x, axis=-1), B_w_gate, B_w_up, B_w_down)
    x = x + _A_ref(_layernorm(x, axis=-1), A_wqkv, A_w_phi, A_wo)
    x = x + _B_ref(_layernorm(x, axis=-1), B_w_gate, B_w_up, B_w_down)
    return x
```

```cpp
#include <hip/hip_runtime.h>
#include <hip/hip_bf16.h>
#include <stdint.h>

constexpr int S_ = 2048, D_ = 640, H_ = 32, DH_ = 20, DF_ = 1728;
constexpr int N3_ = 3 * D_;
constexpr int PHW_ = H_ * 40;

typedef __attribute__((ext_vector_type(8))) short bf16x8;
typedef __attribute__((ext_vector_type(4))) short bf16x4;
typedef __attribute__((ext_vector_type(4))) float f32x4;
typedef __attribute__((ext_vector_type(4))) unsigned int u32x4;
typedef __attribute__((ext_vector_type(2))) unsigned int u32x2;
typedef unsigned short u16;
typedef __attribute__((address_space(3))) bf16x4* lds_b4_ptr;

__device__ __forceinline__ float bf2f(u16 v) { return __uint_as_float(((unsigned)v) << 16); }
__device__ __forceinline__ u16 f2bf(float f) { return __builtin_bit_cast(u16, (__bf16)f); }
__device__ __forceinline__ float rbf(float f) { return bf2f(f2bf(f)); }
__device__ __forceinline__ unsigned pack2(float a, float b) {
    return (unsigned)f2bf(a) | ((unsigned)f2bf(b) << 16);
}
__device__ __forceinline__ bf16x4 tr_read(const char* p) {
    return __builtin_amdgcn_ds_read_tr16_b64_v4i16((lds_b4_ptr)p);
}
__device__ __forceinline__ bf16x8 cat8(bf16x4 a, bf16x4 b) {
    bf16x8 r;
    r[0] = a[0]; r[1] = a[1]; r[2] = a[2]; r[3] = a[3];
    r[4] = b[0]; r[5] = b[1]; r[6] = b[2]; r[7] = b[3];
    return r;
}
#define MFMA(a, b, c) __builtin_amdgcn_mfma_f32_16x16x32_bf16((a), (b), (c), 0, 0, 0)

__device__ __forceinline__ u32x4 ln8(u32x4 v, float mean_bf, float rinv) {
    u32x4 o;
#pragma unroll
    for (int i = 0; i < 4; ++i) {
        float lo = __uint_as_float(v[i] << 16);
        float hi = __uint_as_float(v[i] & 0xffff0000u);
        float a = rbf(lo - mean_bf) * rinv;
        float b = rbf(hi - mean_bf) * rinv;
        o[i] = pack2(a, b);
    }
    return o;
}

__device__ __forceinline__ void row_stats16(const u32x4 (&c)[5], float& mean_bf, float& rinv) {
    float s = 0.f;
#pragma unroll
    for (int i = 0; i < 5; ++i)
#pragma unroll
        for (int j = 0; j < 4; ++j) {
            s += __uint_as_float(c[i][j] << 16);
            s += __uint_as_float(c[i][j] & 0xffff0000u);
        }
    s += __shfl_xor(s, 1); s += __shfl_xor(s, 2); s += __shfl_xor(s, 4); s += __shfl_xor(s, 8);
    float mean = s * (1.0f / 640.0f);
    float q = 0.f;
#pragma unroll
    for (int i = 0; i < 5; ++i)
#pragma unroll
        for (int j = 0; j < 4; ++j) {
            float a = __uint_as_float(c[i][j] << 16) - mean;
            float b = __uint_as_float(c[i][j] & 0xffff0000u) - mean;
            q += a * a; q += b * b;
        }
    q += __shfl_xor(q, 1); q += __shfl_xor(q, 2); q += __shfl_xor(q, 4); q += __shfl_xor(q, 8);
    float var_bf = rbf(q * (1.0f / 640.0f));
    float den = rbf(sqrtf(rbf(var_bf + 1e-5f)));
    mean_bf = rbf(mean);
    rinv = 1.0f / den;
}

template <int NT>
__device__ __forceinline__ int wimg_off(int k, int nch  ) {
    int kb = k >> 3, kr = (k & 7) ^ ((kb & 1) << 2);
    return (kb * NT + (nch >> 1)) * 256 + kr * 32 + (nch & 1) * 16;
}
template <int NT>
__device__ __forceinline__ bf16x8 wfrag(const char* img, int ks, int nt, int lane) {
    int g = lane >> 4, q = (lane & 15) >> 2, p = lane & 3;
    int kb = 4 * ks + g;
    const char* base = img + (kb * NT + nt) * 256 + q * 32 + p * 8;
    int sw = (g & 1) * 128;
    bf16x4 lo = tr_read(base + (0 ^ sw));
    bf16x4 hi = tr_read(base + (128 ^ sw));
    return cat8(lo, hi);
}

constexpr int K1_A = 0;
constexpr int K1_B = 81920;
constexpr int K1_WP = K1_B + 2 * 30720;
constexpr int K1_SMEM = K1_WP + 8192;
constexpr int K1_T = 0;
constexpr int K1_TRS = 144;
constexpr int K1_PK = 256 * K1_TRS;
constexpr int K1_PKRS = 336;

__global__ __launch_bounds__(256) void k1_qkv(const u16* __restrict__ x, const u16* __restrict__ wqkv,
                                              const u16* __restrict__ wphi, u16* __restrict__ phiq,
                                              float* __restrict__ kvp) {
    extern __shared__ __attribute__((aligned(16))) char smem[];
    const int tid = threadIdx.x, lane = tid & 63, wave = tid >> 6;
    const int g = lane >> 4, l15 = lane & 15;
    const int hg = blockIdx.x & 7, rb = blockIdx.x >> 3;
    const int row0 = rb * 64;

    u32x4 breg[8];
    auto bload = [&](int s) {
#pragma unroll
        for (int i = 0; i < 8; ++i) {
            int c = tid + 256 * i;
            if (c < 1920) {
                int k = c / 30, nch = c - k * 30;
                int sec = nch / 10, w = nch - sec * 10;
                breg[i] = *(const u32x4*)(wqkv + (size_t)(s * 64 + k) * N3_ + sec * D_ + hg * 80 + w * 8);
            }
        }
    };
    auto bwrite = [&](int buf) {
#pragma unroll
        for (int i = 0; i < 8; ++i) {
            int c = tid + 256 * i;
            if (c < 1920) {
                int k = c / 30, nch = c - k * 30;
                *(u32x4*)(smem + K1_B + buf * 30720 + wimg_off<15>(k, nch)) = breg[i];
            }
        }
    };
    bload(0);

    {
#pragma unroll
        for (int i = 0; i < 2; ++i) {
            int id = tid + 256 * i;
            int hh = id >> 7, t = (id >> 6) & 1, ln = id & 63;
            int ii = ln & 15, gg = ln >> 4;
            int f = (t == 0) ? (5 * (ii >> 2) + (ii & 3)) : (((ii & 3) == 0) ? (5 * (ii >> 2) + 4) : -1);
            bf16x8 v;
#pragma unroll
            for (int j = 0; j < 8; ++j) {
                int d = 8 * gg + j;
                u16 val = 0;
                if (f >= 0 && d < DH_) val = wphi[((hg * 4 + hh) * DH_ + d) * DH_ + f];
                v[j] = (short)val;
            }
            *(bf16x8*)(smem + K1_WP + id * 16) = v;
        }
    }

#pragma unroll
    for (int it = 0; it < 4; ++it) {
        int r = wave * 16 + it * 4 + g;
        u32x4 c[5];
#pragma unroll
        for (int i = 0; i < 5; ++i) c[i] = *(const u32x4*)(x + (size_t)(row0 + r) * D_ + (l15 + 16 * i) * 8);
        float mb, ri;
        row_stats16(c, mb, ri);
#pragma unroll
        for (int i = 0; i < 5; ++i) {
            int ch = l15 + 16 * i;
            *(u32x4*)(smem + K1_A + r * 1280 + ((ch ^ (r & 15)) << 4)) = ln8(c[i], mb, ri);
        }
    }
    bwrite(0);
    bload(1);
    __syncthreads();

    f32x4 acc[4][4];
#pragma unroll
    for (int i = 0; i < 4; ++i)
#pragma unroll
        for (int j = 0; j < 4; ++j) acc[i][j] = (f32x4){0.f, 0.f, 0.f, 0.f};
    const int nct = (wave == 3) ? 3 : 4;
    for (int s = 0; s < 10; ++s) {
        const char* bimg = smem + K1_B + (s & 1) * 30720;
#pragma unroll
        for (int ks = 0; ks < 2; ++ks) {
            bf16x8 a[4];
#pragma unroll
            for (int rt = 0; rt < 4; ++rt) {
                int r = rt * 16 + l15;
                int ch = s * 8 + ks * 4 + g;
                a[rt] = *(const bf16x8*)(smem + K1_A + r * 1280 + ((ch ^ (r & 15)) << 4));
            }
#pragma unroll
            for (int ct = 0; ct < 4; ++ct) {
                if (ct < nct) {
                    bf16x8 b = wfrag<15>(bimg, ks, wave * 4 + ct, lane);
#pragma unroll
                    for (int rt = 0; rt < 4; ++rt) acc[rt][ct] = MFMA(a[rt], b, acc[rt][ct]);
                }
            }
        }
        if (s + 1 < 10) bwrite((s + 1) & 1);
        if (s + 2 < 10) bload(s + 2);
        __syncthreads();
    }

#pragma unroll
    for (int ct = 0; ct < 4; ++ct) {
        if (ct < nct) {
            int col = (wave * 4 + ct) * 16 + l15;
#pragma unroll
            for (int rt = 0; rt < 4; ++rt) {
                u32x2 v;
                v[0] = pack2(acc[rt][ct][0], acc[rt][ct][1]);
                v[1] = pack2(acc[rt][ct][2], acc[rt][ct][3]);
                *(u32x2*)(smem + K1_T + col * K1_TRS + (rt * 16 + 4 * g) * 2) = v;
            }
        }
    }
    for (int i = tid; i < 16 * 32; i += 256) {
        int c = 240 + (i >> 5), w = i & 31;
        *(unsigned*)(smem + K1_T + c * K1_TRS + w * 4) = (c == 240) ? 0x3f803f80u : 0u;
    }
    __syncthreads();

    {
        const int q4 = l15 >> 2, p4 = lane & 3;
        const int rowt = wave * 16;
#pragma unroll
        for (int hh = 0; hh < 4; ++hh) {
            bf16x8 wp0 = *(const bf16x8*)(smem + K1_WP + ((hh * 2 + 0) * 64 + lane) * 16);
            bf16x8 wp1 = *(const bf16x8*)(smem + K1_WP + ((hh * 2 + 1) * 64 + lane) * 16);
#pragma unroll
            for (int sec = 0; sec < 2; ++sec) {
                int c0 = sec * 80 + hh * 20 + 8 * g + q4;
                const char* tb = smem + K1_T + c0 * K1_TRS + (rowt + 4 * p4) * 2;
                bf16x8 b = cat8(tr_read(tb), tr_read(tb + 4 * K1_TRS));
                f32x4 z = {0.f, 0.f, 0.f, 0.f};
                f32x4 d0 = MFMA(wp0, b, z);
                f32x4 d1 = MFMA(wp1, b, z);
                float p[5];
                p[0] = rbf(d0[0]); p[1] = rbf(d0[1]); p[2] = rbf(d0[2]); p[3] = rbf(d0[3]); p[4] = rbf(d1[0]);
                float mx = fmaxf(fmaxf(fmaxf(p[0], p[1]), fmaxf(p[2], p[3])), p[4]);
                float mn = fminf(fminf(fminf(p[0], p[1]), fminf(p[2], p[3])), p[4]);
                mx = fmaxf(mx, __shfl_xor(mx, 16)); mx = fmaxf(mx, __shfl_xor(mx, 32));
                mn = fminf(mn, __shfl_xor(mn, 16)); mn = fminf(mn, __shfl_xor(mn, 32));
                float ep[5], en[5], sp = 0.f, sn = 0.f;
#pragma unroll
                for (int i = 0; i < 5; ++i) {
                    ep[i] = __expf(p[i] - mx); sp += ep[i];
                    en[i] = __expf(mn - p[i]); sn += en[i];
                }
                sp += __shfl_xor(sp, 16); sp += __shfl_xor(sp, 32);
                sn += __shfl_xor(sn, 16); sn += __shfl_xor(sn, 32);
                float ip = 1.0f / sp, in_ = 1.0f / sn;
                unsigned o[5];
                o[0] = pack2(ep[0] * ip, ep[1] * ip);
                o[1] = pack2(ep[2] * ip, ep[3] * ip);
                o[2] = pack2(ep[4] * ip, en[0] * in_);
                o[3] = pack2(en[1] * in_, en[2] * in_);
                o[4] = pack2(en[3] * in_, en[4] * in_);
                if (sec == 0) {
                    unsigned* dst = (unsigned*)(phiq + (size_t)(row0 + rowt + l15) * PHW_ + (hg * 4 + hh) * 40 + 10 * g);
#pragma unroll
                    for (int i = 0; i < 5; ++i) dst[i] = o[i];
                } else {
                    unsigned* dst = (unsigned*)(smem + K1_PK + (rowt + l15) * K1_PKRS + hh * 80 + 20 * g);
#pragma unroll
                    for (int i = 0; i < 5; ++i) dst[i] = o[i];
                }
            }
        }
    }
    __syncthreads();

    {
        const int hh = wave;
        const int q4 = l15 >> 2, p4 = lane & 3;
        f32x4 kacc[3][2];
#pragma unroll
        for (int ft = 0; ft < 3; ++ft)
#pragma unroll
            for (int dt = 0; dt < 2; ++dt) kacc[ft][dt] = (f32x4){0.f, 0.f, 0.f, 0.f};
#pragma unroll
        for (int ks = 0; ks < 2; ++ks) {
            bf16x8 b[2];
#pragma unroll
            for (int dt = 0; dt < 2; ++dt) {
                int c = 160 + hh * 20 + dt * 16 + l15;
                if (dt == 1 && l15 >= 4) c = 240;
                b[dt] = *(const bf16x8*)(smem + K1_T + c * K1_TRS + (32 * ks + 8 * g) * 2);
            }
#pragma unroll
            for (int ft = 0; ft < 3; ++ft) {
                const char* pb = smem + K1_PK + (32 * ks + 8 * g + q4) * K1_PKRS + (hh * 40 + 16 * ft + 4 * p4) * 2;
                bf16x8 a = cat8(tr_read(pb), tr_read(pb + 4 * K1_PKRS));
#pragma unroll
                for (int dt = 0; dt < 2; ++dt) kacc[ft][dt] = MFMA(a, b[dt], kacc[ft][dt]);
            }
        }
        float* dst = kvp + (size_t)(rb * H_ + hg * 4 + hh) * 40 * 32;
#pragma unroll
        for (int ft = 0; ft < 3; ++ft)
#pragma unroll
            for (int j = 0; j < 4; ++j) {
                int f = 16 * ft + 4 * g + j;
                if (f < 40) {
#pragma unroll
                    for (int dt = 0; dt < 2; ++dt) dst[f * 32 + dt * 16 + l15] = kacc[ft][dt][j];
                }
            }
    }
}

__global__ __launch_bounds__(256) void kr_reduce(const float* __restrict__ kvp, u16* __restrict__ kvt) {
    int id = blockIdx.x * 256 + threadIdx.x;
    int d = id & 31, f = (id >> 5) & 63, h = id >> 11;
    float s = 0.f;
    if (f < 40 && d < 21) {
#pragma unroll 8
        for (int rb = 0; rb < 32; ++rb) s += kvp[((size_t)(rb * H_ + h) * 40 + f) * 32 + d];
    }
    kvt[(h * 32 + d) * 64 + f] = f2bf(s);
}

constexpr int K2_O = 0;
constexpr int K2_B = 81920;
constexpr int K2_SMEM = K2_B + 2 * 10240;

__global__ __launch_bounds__(256) void k2_attn_out(const u16* __restrict__ x, const u16* __restrict__ phiq,
                                                   const u16* __restrict__ kvt, const u16* __restrict__ wo,
                                                   u16* __restrict__ xout) {
    extern __shared__ __attribute__((aligned(16))) char smem[];
    const int tid = threadIdx.x, lane = tid & 63, wave = tid >> 6;
    const int g = lane >> 4, l15 = lane & 15;
    const int cb = blockIdx.x & 7, rb = blockIdx.x >> 3;
    const int row0 = rb * 64, col0 = cb * 80;

    u32x4 breg[3];
    auto bload = [&](int s) {
#pragma unroll
        for (int i = 0; i < 3; ++i) {
            int c = tid + 256 * i;
            if (c < 640) {
                int k = c / 10, nch = c - k * 10;
                breg[i] = *(const u32x4*)(wo + (size_t)(s * 64 + k) * D_ + col0 + nch * 8);
            }
        }
    };
    auto bwrite = [&](int buf) {
#pragma unroll
        for (int i = 0; i < 3; ++i) {
            int c = tid + 256 * i;
            if (c < 640) {
                int k = c / 10, nch = c - k * 10;
                *(u32x4*)(smem + K2_B + buf * 10240 + wimg_off<5>(k, nch)) = breg[i];
            }
        }
    };
    bload(0);

    for (int hi = 0; hi < 8; ++hi) {
        int h = wave * 8 + hi;
        bf16x8 a[2][2];
#pragma unroll
        for (int dt = 0; dt < 2; ++dt)
#pragma unroll
            for (int ks = 0; ks < 2; ++ks)
                a[dt][ks] = *(const bf16x8*)(kvt + (h * 32 + 16 * dt + l15) * 64 + 32 * ks + 8 * g);
#pragma unroll
        for (int rt = 0; rt < 4; ++rt) {
            int r = rt * 16 + l15;
            const u16* pr = phiq + (size_t)(row0 + r) * PHW_ + h * 40;
            bf16x8 b0 = *(const bf16x8*)(pr + 8 * g);
            bf16x8 b1 = *(const bf16x8*)(pr + 32);
            if (g != 0) b1 = (bf16x8){0, 0, 0, 0, 0, 0, 0, 0};
            f32x4 z = {0.f, 0.f, 0.f, 0.f};
            f32x4 d0 = MFMA(a[0][0], b0, z); d0 = MFMA(a[0][1], b1, d0);
            f32x4 d1 = MFMA(a[1][0], b0, z); d1 = MFMA(a[1][1], b1, d1);
            float den = __shfl(d1[0], 16 + l15);
            den = rbf(rbf(den) + 1e-6f);
            u32x2 v;
            v[0] = pack2(rbf(d0[0]) / den, rbf(d0[1]) / den);
            v[1] = pack2(rbf(d0[2]) / den, rbf(d0[3]) / den);
            int bo = (h * 20 + 4 * g) * 2;
            *(u32x2*)(smem + K2_O + r * 1280 + (((bo >> 4) ^ (r & 15)) << 4) + (bo & 15)) = v;
            if (g == 0) {
                v[0] = pack2(rbf(d1[0]) / den, rbf(d1[1]) / den);
                v[1] = pack2(rbf(d1[2]) / den, rbf(d1[3]) / den);
                bo = (h * 20 + 16) * 2;
                *(u32x2*)(smem + K2_O + r * 1280 + (((bo >> 4) ^ (r & 15)) << 4) + (bo & 15)) = v;
            }
        }
    }
    bwrite(0);
    bload(1);
    __syncthreads();

    f32x4 acc[5];
#pragma unroll
    for (int i = 0; i < 5; ++i) acc[i] = (f32x4){0.f, 0.f, 0.f, 0.f};
    for (int s = 0; s < 10; ++s) {
        const char* bimg = smem + K2_B + (s & 1) * 10240;
#pragma unroll
        for (int ks = 0; ks < 2; ++ks) {
            int r = wave * 16 + l15;
            int ch = s * 8 + ks * 4 + g;
            bf16x8 a = *(const bf16x8*)(smem + K2_O + r * 1280 + ((ch ^ (r & 15)) << 4));
#pragma unroll
            for (int ct = 0; ct < 5; ++ct) {
                bf16x8 b = wfrag<5>(bimg, ks, ct, lane);
                acc[ct] = MFMA(b, a, acc[ct]);
            }
        }
        if (s + 1 < 10) bwrite((s + 1) & 1);
        if (s + 2 < 10) bload(s + 2);
        __syncthreads();
    }
    {
        int row = row0 + wave * 16 + l15;
#pragma unroll
        for (int ct = 0; ct < 5; ++ct) {
            int col = col0 + ct * 16 + 4 * g;
            u32x2 xv = *(const u32x2*)(x + (size_t)row * D_ + col);
            u32x2 v;
            v[0] = pack2(__uint_as_float(xv[0] << 16) + rbf(acc[ct][0]), __uint_as_float(xv[0] & 0xffff0000u) + rbf(acc[ct][1]));
            v[1] = pack2(__uint_as_float(xv[1] << 16) + rbf(acc[ct][2]), __uint_as_float(xv[1] & 0xffff0000u) + rbf(acc[ct][3]));
            *(u32x2*)(xout + (size_t)row * D_ + col) = v;
        }
    }
}

constexpr int K3_A = 0;
constexpr int K3_B = 65536;
constexpr int K3_ST = K3_B + 32768;
constexpr int K3_SMEM = K3_ST + 2048;

__device__ __forceinline__ float gelu_tanh(float x) {
    float y = 0.7978845608028654f * (x + 0.044715f * x * x * x);
    float t = 1.0f - 2.0f / (1.0f + __expf(2.0f * y));
    return 0.5f * x * (1.0f + t);
}

__global__ __launch_bounds__(512) void k3_gate_up(const u16* __restrict__ x, const u16* __restrict__ wg,
                                                  const u16* __restrict__ wu, u16* __restrict__ hbuf) {
    extern __shared__ __attribute__((aligned(16))) char smem[];
    const int tid = threadIdx.x, lane = tid & 63, wave = tid >> 6;
    const int g = lane >> 4, l15 = lane & 15;
    const int rb = blockIdx.x & 7, cb = blockIdx.x >> 3;
    const int row0 = rb * 256, col0 = cb * 64;
    float2* stats = (float2*)(smem + K3_ST);

    u32x4 areg[4], breg[2];
    auto gload = [&](int s) {
#pragma unroll
        for (int i = 0; i < 4; ++i) {
            int c = tid + 512 * i;
            int r = c >> 3, ch = c & 7;
            areg[i] = *(const u32x4*)(x + (size_t)(row0 + r) * D_ + s * 64 + ch * 8);
        }
#pragma unroll
        for (int i = 0; i < 2; ++i) {
            int c = tid + 512 * i;
            int k = c >> 4, nch = c & 15;
            const u16* w = (nch < 8) ? wg : wu;
            breg[i] = *(const u32x4*)(w + (size_t)(s * 64 + k) * DF_ + col0 + (nch & 7) * 8);
        }
    };
    auto swrite = [&](int buf) {
#pragma unroll
        for (int i = 0; i < 4; ++i) {
            int c = tid + 512 * i;
            int r = c >> 3, ch = c & 7;
            float2 st = stats[r];
            *(u32x4*)(smem + K3_A + buf * 32768 + r * 128 + ((ch ^ (r & 7)) << 4)) = ln8(areg[i], st.x, st.y);
        }
#pragma unroll
        for (int i = 0; i < 2; ++i) {
            int c = tid + 512 * i;
            int k = c >> 4, nch = c & 15;
            *(u32x4*)(smem + K3_B + buf * 16384 + wimg_off<8>(k, nch)) = breg[i];
        }
    };
    gload(0);
#pragma unroll 2
    for (int it = 0; it < 8; ++it) {
        int r = wave * 32 + it * 4 + g;
        u32x4 c[5];
#pragma unroll
        for (int i = 0; i < 5; ++i) c[i] = *(const u32x4*)(x + (size_t)(row0 + r) * D_ + (l15 + 16 * i) * 8);
        float mb, ri;
        row_stats16(c, mb, ri);
        if (l15 == 0) stats[r] = make_float2(mb, ri);
    }
    __syncthreads();
    swrite(0);
    gload(1);
    __syncthreads();

    const int wr = wave >> 1, wc = wave & 1;
    f32x4 acc[4][4];
#pragma unroll
    for (int i = 0; i < 4; ++i)
#pragma unroll
        for (int j = 0; j < 4; ++j) acc[i][j] = (f32x4){0.f, 0.f, 0.f, 0.f};
    for (int s = 0; s < 10; ++s) {
        const char* aimg = smem + K3_A + (s & 1) * 32768;
        const char* bimg = smem + K3_B + (s & 1) * 16384;
#pragma unroll
        for (int ks = 0; ks < 2; ++ks) {
            bf16x8 a[4];
#pragma unroll
            for (int rt = 0; rt < 4; ++rt) {
                int r = wr * 64 + rt * 16 + l15;
                int ch = ks * 4 + g;
                a[rt] = *(const bf16x8*)(aimg + r * 128 + ((ch ^ (r & 7)) << 4));
            }
#pragma unroll
            for (int ct = 0; ct < 4; ++ct) {
                int nt = (ct >> 1) * 4 + wc * 2 + (ct & 1);
                bf16x8 b = wfrag<8>(bimg, ks, nt, lane);
#pragma unroll
                for (int rt = 0; rt < 4; ++rt) acc[rt][ct] = MFMA(b, a[rt], acc[rt][ct]);
            }
        }
        if (s + 1 < 10) swrite((s + 1) & 1);
        if (s + 2 < 10) gload(s + 2);
        __syncthreads();
    }
#pragma unroll
    for (int rt = 0; rt < 4; ++rt) {
        int row = row0 + wr * 64 + rt * 16 + l15;
#pragma unroll
        for (int c2 = 0; c2 < 2; ++c2) {
            int col = col0 + (wc * 2 + c2) * 16 + 4 * g;
            float hv[4];
#pragma unroll
            for (int j = 0; j < 4; ++j) {
                float gg = rbf(acc[rt][c2][j]);
                float uu = rbf(acc[rt][2 + c2][j]);
                hv[j] = rbf(gelu_tanh(gg)) * uu;
            }
            u32x2 v;
            v[0] = pack2(hv[0], hv[1]);
            v[1] = pack2(hv[2], hv[3]);
            *(u32x2*)(hbuf + (size_t)row * DF_ + col) = v;
        }
    }
}

constexpr int K4_A = 0;
constexpr int K4_B = 16384;
constexpr int K4_SMEM = K4_B + 2 * 10240;

__global__ __launch_bounds__(256) void k4_down(const u16* __restrict__ hbuf, const u16* __restrict__ wd,
                                               const u16* __restrict__ xin, u16* __restrict__ xout) {
    extern __shared__ __attribute__((aligned(16))) char smem[];
    const int tid = threadIdx.x, lane = tid & 63, wave = tid >> 6;
    const int g = lane >> 4, l15 = lane & 15;
    const int cb = blockIdx.x & 7, rb = blockIdx.x >> 3;
    const int row0 = rb * 64, col0 = cb * 80;
    constexpr int NS = DF_ / 64;

    u32x4 areg[2], breg[3];
    auto gload = [&](int s) {
#pragma unroll
        for (int i = 0; i < 2; ++i) {
            int c = tid + 256 * i;
            int r = c >> 3, ch = c & 7;
            areg[i] = *(const u32x4*)(hbuf + (size_t)(row0 + r) * DF_ + s * 64 + ch * 8);
        }
#pragma unroll
        for (int i = 0; i < 3; ++i) {
            int c = tid + 256 * i;
            if (c < 640) {
                int k = c / 10, nch = c - k * 10;
                breg[i] = *(const u32x4*)(wd + (size_t)(s * 64 + k) * D_ + col0 + nch * 8);
            }
        }
    };
    auto swrite = [&](int buf) {
#pragma unroll
        for (int i = 0; i < 2; ++i) {
            int c = tid + 256 * i;
            int r = c >> 3, ch = c & 7;
            *(u32x4*)(smem + K4_A + buf * 8192 + r * 128 + ((ch ^ (r & 7)) << 4)) = areg[i];
        }
#pragma unroll
        for (int i = 0; i < 3; ++i) {
            int c = tid + 256 * i;
            if (c < 640) {
                int k = c / 10, nch = c - k * 10;
                *(u32x4*)(smem + K4_B + buf * 10240 + wimg_off<5>(k, nch)) = breg[i];
            }
        }
    };
    gload(0);
    swrite(0);
    gload(1);
    __syncthreads();

    f32x4 acc[5];
#pragma unroll
    for (int i = 0; i < 5; ++i) acc[i] = (f32x4){0.f, 0.f, 0.f, 0.f};
    for (int s = 0; s < NS; ++s) {
        const char* aimg = smem + K4_A + (s & 1) * 8192;
        const char* bimg = smem + K4_B + (s & 1) * 10240;
#pragma unroll
        for (int ks = 0; ks < 2; ++ks) {
            int r = wave * 16 + l15;
            int ch = ks * 4 + g;
            bf16x8 a = *(const bf16x8*)(aimg + r * 128 + ((ch ^ (r & 7)) << 4));
#pragma unroll
            for (int ct = 0; ct < 5; ++ct) {
                bf16x8 b = wfrag<5>(bimg, ks, ct, lane);
                acc[ct] = MFMA(b, a, acc[ct]);
            }
        }
        if (s + 1 < NS) swrite((s + 1) & 1);
        if (s + 2 < NS) gload(s + 2);
        __syncthreads();
    }
    {
        int row = row0 + wave * 16 + l15;
#pragma unroll
        for (int ct = 0; ct < 5; ++ct) {
            int col = col0 + ct * 16 + 4 * g;
            u32x2 xv = *(const u32x2*)(xin + (size_t)row * D_ + col);
            u32x2 v;
            v[0] = pack2(__uint_as_float(xv[0] << 16) + rbf(acc[ct][0]), __uint_as_float(xv[0] & 0xffff0000u) + rbf(acc[ct][1]));
            v[1] = pack2(__uint_as_float(xv[1] << 16) + rbf(acc[ct][2]), __uint_as_float(xv[1] & 0xffff0000u) + rbf(acc[ct][3]));
            *(u32x2*)(xout + (size_t)row * D_ + col) = v;
        }
    }
}

extern "C" void kernel_launch(void* const* d_in, const int* in_sizes, int n_in,
                              void* d_out, int out_size, void* d_ws, size_t ws_size,
                              hipStream_t stream) {
    const u16* A_x = (const u16*)d_in[0];
    const u16* wqkv = (const u16*)d_in[1];
    const u16* wphi = (const u16*)d_in[2];
    const u16* wo = (const u16*)d_in[3];
    const u16* wg = (const u16*)d_in[5];
    const u16* wu = (const u16*)d_in[6];
    const u16* wd = (const u16*)d_in[7];
    u16* out = (u16*)d_out;

    char* ws = (char*)d_ws;
    u16* x1 = (u16*)(ws);
    u16* x2 = (u16*)(ws + (4u << 20));
    u16* phiq = (u16*)(ws + (8u << 20));
    float* kvp = (float*)(ws + (16u << 20));
    u16* kvt = (u16*)(ws + (24u << 20));
    u16* hbuf = (u16*)(ws + (28u << 20));

    hipFuncSetAttribute((const void*)k1_qkv, hipFuncAttributeMaxDynamicSharedMemorySize, K1_SMEM);
    hipFuncSetAttribute((const void*)k2_attn_out, hipFuncAttributeMaxDynamicSharedMemorySize, K2_SMEM);
    hipFuncSetAttribute((const void*)k3_gate_up, hipFuncAttributeMaxDynamicSharedMemorySize, K3_SMEM);
    hipFuncSetAttribute((const void*)k4_down, hipFuncAttributeMaxDynamicSharedMemorySize, K4_SMEM);

    const u16* xin = A_x;
    for (int layer = 0; layer < 2; ++layer) {
        u16* xmid = x1;
        u16* xo = (layer == 0) ? x2 : out;
        k1_qkv<<<256, 256, K1_SMEM, stream>>>(xin, wqkv, wphi, phiq, kvp);
        kr_reduce<<<256, 256, 0, stream>>>(kvp, kvt);
        k2_attn_out<<<256, 256, K2_SMEM, stream>>>(xin, phiq, kvt, wo, xmid);
        k3_gate_up<<<216, 512, K3_SMEM, stream>>>(xmid, wg, wu, hbuf);
        k4_down<<<256, 256, K4_SMEM, stream>>>(hbuf, wd, xmid, xo);
        xin = x2;
    }
}
```

```cpp
#include <hip/hip_runtime.h>
#include <hip/hip_bf16.h>
#include <stdint.h>

#define STAMP(k, i)
constexpr int S_ = 2048, D_ = 640, H_ = 32, DH_ = 20, DF_ = 1728;
constexpr int N3_ = 3 * D_;

typedef __attribute__((ext_vector_type(8))) short bf16x8;
typedef __attribute__((ext_vector_type(4))) short bf16x4;
typedef __attribute__((ext_vector_type(4))) float f32x4;
typedef __attribute__((ext_vector_type(4))) unsigned int u32x4;
typedef __attribute__((ext_vector_type(2))) unsigned int u32x2;
typedef unsigned short u16;
typedef __attribute__((address_space(3))) bf16x4* lds_b4_ptr;
typedef __attribute__((address_space(3))) void* lds_vptr;
typedef const __attribute__((address_space(1))) void* glb_vptr;

__device__ __forceinline__ float bf2f(u16 v) { return __uint_as_float(((unsigned)v) << 16); }
__device__ __forceinline__ u16 f2bf(float f) { return __builtin_bit_cast(u16, (__bf16)f); }
__device__ __forceinline__ float rbf(float f) { return bf2f(f2bf(f)); }
__device__ __forceinline__ unsigned pack2(float a, float b) { return (unsigned)f2bf(a) | ((unsigned)f2bf(b) << 16); }
__device__ __forceinline__ float lo16(unsigned u) { return __uint_as_float(u << 16); }
__device__ __forceinline__ float hi16(unsigned u) { return __uint_as_float(u & 0xffff0000u); }
__device__ __forceinline__ bf16x4 tr_read(const char* p) { return __builtin_amdgcn_ds_read_tr16_b64_v4i16((lds_b4_ptr)p); }
__device__ __forceinline__ bf16x8 cat8v(bf16x4 a, bf16x4 b) { return __builtin_shufflevector(a, b, 0, 1, 2, 3, 4, 5, 6, 7); }
#define MFMA(a, b, c) __builtin_amdgcn_mfma_f32_16x16x32_bf16((a), (b), (c), 0, 0, 0)

template <int CTRL> __device__ __forceinline__ float dpp_mov(float x) {
    return __int_as_float(__builtin_amdgcn_update_dpp(0, __float_as_int(x), CTRL, 0xf, 0xf, false));
}
__device__ __forceinline__ float sum16(float x) {
    x += dpp_mov<0x128>(x); x += dpp_mov<0x124>(x); x += dpp_mov<0x122>(x); x += dpp_mov<0x121>(x);
    return x;
}
__device__ __forceinline__ void swap16(float x, float& a, float& b) {
    auto r = __builtin_amdgcn_permlane16_swap(__float_as_uint(x), __float_as_uint(x), false, false);
    a = __uint_as_float(r[0]); b = __uint_as_float(r[1]);
}
__device__ __forceinline__ void swap32(float x, float& a, float& b) {
    auto r = __builtin_amdgcn_permlane32_swap(__float_as_uint(x), __float_as_uint(x), false, false);
    a = __uint_as_float(r[0]); b = __uint_as_float(r[1]);
}
__device__ __forceinline__ float sum_g(float x) { float a, b; swap16(x, a, b); x = a + b; swap32(x, a, b); return a + b; }
__device__ __forceinline__ float max_g(float x) { float a, b; swap16(x, a, b); x = fmaxf(a, b); swap32(x, a, b); return fmaxf(a, b); }
__device__ __forceinline__ float min_g(float x) { float a, b; swap16(x, a, b); x = fminf(a, b); swap32(x, a, b); return fminf(a, b); }

__device__ __forceinline__ u32x4 ln8(u32x4 v, float mean_bf, float rinv) {
    u32x4 o;
#pragma unroll
    for (int i = 0; i < 4; ++i) {
        float a = rbf(lo16(v[i]) - mean_bf) * rinv;
        float b = rbf(hi16(v[i]) - mean_bf) * rinv;
        o[i] = pack2(a, b);
    }
    return o;
}
__device__ __forceinline__ void row_stats16(const u32x4 (&c)[5], float& mean_bf, float& rinv) {
    float s = 0.f;
#pragma unroll
    for (int i = 0; i < 5; ++i)
#pragma unroll
        for (int j = 0; j < 4; ++j) { s += lo16(c[i][j]); s += hi16(c[i][j]); }
    s = sum16(s);
    float mean = s * (1.0f / 640.0f);
    float q = 0.f;
#pragma unroll
    for (int i = 0; i < 5; ++i)
#pragma unroll
        for (int j = 0; j < 4; ++j) {
            float a = lo16(c[i][j]) - mean, b = hi16(c[i][j]) - mean;
            q += a * a; q += b * b;
        }
    q = sum16(q);
    float var_bf = rbf(q * (1.0f / 640.0f));
    float den = rbf(sqrtf(rbf(var_bf + 1e-5f)));
    mean_bf = rbf(mean);
    rinv = 1.0f / den;
}

#define RAW_BAR() do { asm volatile("s_waitcnt lgkmcnt(0)" ::: "memory"); __builtin_amdgcn_s_barrier(); asm volatile("" ::: "memory"); } while (0)
__device__ __forceinline__ void glds16(const void* src, char* lds_dst) {
    __builtin_amdgcn_global_load_lds((glb_vptr)src, (lds_vptr)lds_dst, 16, 0, 0);
}
template <int CPR>
__device__ __forceinline__ void wsrc(int ins, int lane, int& k, int& nch) {
    int c = ins * 64 + lane;
    int pos = c / CPR;
    nch = c - pos * CPR;
    k = pos ^ (((pos >> 3) & 1) << 2);
}
#define LDS_TR(dst, addr, off) asm volatile("ds_read_b64_tr_b16 %0, %1 offset:%2" : "=v"(dst) : "v"(addr), "i"(off))
#define LDS_B128(dst, addr, off) asm volatile("ds_read_b128 %0, %1 offset:%2" : "=v"(dst) : "v"(addr), "i"(off))
__device__ __forceinline__ unsigned lds_addr(const void* p) { return (unsigned)(size_t)p; }
template <int RS>
__device__ __forceinline__ void wfrag_addr(unsigned img, int lane, unsigned& lo, unsigned& hi) {
    int g = lane >> 4, q = (lane & 15) >> 2, p = lane & 3;
    int sw = (g & 1) * 4;
    lo = img + (8 * g + sw + q) * RS + p * 8;
    hi = img + (8 * g + (4 ^ sw) + q) * RS + p * 8;
}
template <int N> __device__ __forceinline__ void wait_vm() { asm volatile("s_waitcnt vmcnt(%0)" ::"n"(N) : "memory"); }
template <int PER, int MAXS>
__device__ __forceinline__ void wait_stages(int stages) {
    if (stages >= MAXS) { wait_vm<PER * MAXS>(); return; }
    if constexpr (MAXS > 0) wait_stages<PER, MAXS - 1>(stages);
}

#define BAR_ONLY() do { asm volatile("" ::: "memory"); __builtin_amdgcn_s_barrier(); asm volatile("" ::: "memory"); } while (0)
#define SB0() __builtin_amdgcn_sched_barrier(0)

constexpr int PHT_ = 1280;
constexpr int KVT_H = 21 * 40;

constexpr int K1_A = 0;
constexpr int K1_B = 81920;
constexpr int K1_STG = 15360;
constexpr int K1_WP = K1_B + 4 * K1_STG;
constexpr int K1_SMEM = K1_WP + 8192;
constexpr int K1_T = 0;
constexpr int K1_TRS = 144;
constexpr int K1_PK = 256 * K1_TRS;
constexpr int K1_PKRS = 336;

struct Frag2x4 { bf16x8 a[2]; bf16x4 bl[4], bh[4]; };
#define K1_READ_CT(N, ct, so) do { LDS_TR(N.bl[ct], blo + (so), ct * 32); LDS_TR(N.bh[ct], bhi + (so), ct * 32); } while (0)
#define K1_WAIT(F)                                                                                      \
    asm volatile("s_waitcnt lgkmcnt(0)"                                                                 \
                 : "+v"(F.a[0]), "+v"(F.a[1]), "+v"(F.bl[0]), "+v"(F.bl[1]), "+v"(F.bl[2]), "+v"(F.bl[3]), \
                   "+v"(F.bh[0]), "+v"(F.bh[1]), "+v"(F.bh[2]), "+v"(F.bh[3]))
#define K1_READ(N, AA, SO)                                                                              \
    do {                                                                                                \
        const unsigned aa_ = (AA), so_ = (SO);                                                          \
        LDS_B128(N.a[0], aa_, 0); LDS_B128(N.a[1], aa_, 16 * 1280);                                     \
        K1_READ_CT(N, 0, so_); K1_READ_CT(N, 1, so_); K1_READ_CT(N, 2, so_); K1_READ_CT(N, 3, so_);     \
    } while (0)
#define K1_MMA_CT(C, ct)                                                                                \
    do {                                                                                                \
        bf16x8 b_ = cat8v(C.bl[ct], C.bh[ct]);                                                          \
        acc[0][ct] = MFMA(C.a[0], b_, acc[0][ct]);                                                      \
        acc[1][ct] = MFMA(C.a[1], b_, acc[1][ct]);                                                      \
    } while (0)
#define K1_STEP(C, N, AA, SO)                                                                           \
    do {                                                                                                \
        const unsigned aa_ = (AA), so_ = (SO);                                                          \
        LDS_B128(N.a[0], aa_, 0); K1_READ_CT(N, 0, so_); SB0(); K1_MMA_CT(C, 0); SB0();                 \
        LDS_B128(N.a[1], aa_, 16 * 1280); K1_READ_CT(N, 1, so_); SB0(); K1_MMA_CT(C, 1); SB0();         \
        K1_READ_CT(N, 2, so_); SB0(); K1_MMA_CT(C, 2); SB0();                                           \
        K1_READ_CT(N, 3, so_); SB0(); K1_MMA_CT(C, 3); SB0();                                           \
    } while (0)

__global__ __launch_bounds__(768, 3) void k1_qkv(const u16* __restrict__ x, const u16* __restrict__ wqkv,
                                                 const u16* __restrict__ wphi, char* __restrict__ phiq,
                                                 float* __restrict__ kvp) {
    extern __shared__ __attribute__((aligned(16))) char smem[];
    const int tid = threadIdx.x, lane = tid & 63, wave = __builtin_amdgcn_readfirstlane(tid >> 6);
    const int g = lane >> 4, l15 = lane & 15;
    const int hg = blockIdx.x & 7, rb = blockIdx.x >> 3;
    const int row0 = rb * 64;
    STAMP(0, 0);

    if (wave >= 8) {
        const int lw = wave - 8;
        const u16* src[4];
#pragma unroll
        for (int i = 0; i < 4; ++i) {
            int ins = lw + 4 * i; if (ins > 14) ins = 14;
            int k, nch;
            wsrc<30>(ins, lane, k, nch);
            int sec = nch / 10, w = nch - sec * 10;
            src[i] = wqkv + (size_t)k * N3_ + sec * D_ + hg * 80 + w * 8;
        }
        auto stage = [&](int s) {
            char* base = smem + K1_B + (s & 3) * K1_STG;
#pragma unroll
            for (int i = 0; i < 3; ++i) glds16(src[i] + (size_t)s * 32 * N3_, base + (lw + 4 * i) * 1024);
            if (lw < 3) glds16(src[3] + (size_t)s * 32 * N3_, base + (lw + 12) * 1024);
        };
        stage(0); stage(1); stage(2);
#pragma unroll 1
        for (int b = 0; b < 20; ++b) {
            if (lw < 3) wait_stages<4, 2>(19 - b); else wait_stages<3, 2>(19 - b);
            BAR_ONLY();
            if (b + 3 < 20) stage(b + 3);
        }
        return;
    }

    u32x4 xc[2][5];
#pragma unroll
    for (int it = 0; it < 2; ++it) {
        int r = wave * 8 + it * 4 + g;
#pragma unroll
        for (int i = 0; i < 5; ++i) xc[it][i] = *(const u32x4*)(x + (size_t)(row0 + r) * D_ + (l15 + 16 * i) * 8);
    }
    {
        int hh = tid >> 7, t = (tid >> 6) & 1;
        int wf = (t == 0) ? (5 * (l15 >> 2) + (l15 & 3)) : (((l15 & 3) == 0) ? (5 * (l15 >> 2) + 4) : -1);
        int fc = wf < 0 ? 0 : wf;
        u16 wpv[8];
#pragma unroll
        for (int j = 0; j < 8; ++j) {
            int d = 8 * g + j; if (d > 19) d = 19;
            wpv[j] = wphi[((hg * 4 + hh) * DH_ + d) * DH_ + fc];
        }
        bf16x8 v;
#pragma unroll
        for (int j = 0; j < 8; ++j) v[j] = (wf >= 0 && 8 * g + j < DH_) ? (short)wpv[j] : (short)0;
        *(bf16x8*)(smem + K1_WP + tid * 16) = v;
    }
#pragma unroll
    for (int it = 0; it < 2; ++it) {
        int r = wave * 8 + it * 4 + g;
        float mb, ri;
        row_stats16(xc[it], mb, ri);
#pragma unroll
        for (int i = 0; i < 5; ++i) {
            int ch = l15 + 16 * i;
            *(u32x4*)(smem + K1_A + r * 1280 + ((ch ^ (r & 15)) << 4)) = ln8(xc[it][i], mb, ri);
        }
    }
    asm volatile("s_waitcnt lgkmcnt(0)" ::: "memory");
    STAMP(0, 1);

    const int wr = wave >> 2, wc = wave & 3;
    f32x4 acc[2][4];
#pragma unroll
    for (int i = 0; i < 2; ++i)
#pragma unroll
        for (int j = 0; j < 4; ++j) acc[i][j] = (f32x4){0.f, 0.f, 0.f, 0.f};
    const int nct = (wc == 3) ? 3 : 4;
    {
        const unsigned sm = lds_addr(smem);
        unsigned blo, bhi;
        wfrag_addr<480>(sm + K1_B + wc * 4 * 32, lane, blo, bhi);
        const unsigned aad = sm + K1_A + (wr * 32 + l15) * 1280;
#define K1_AA(s) (aad + (((unsigned)(((s) * 4 + g) ^ l15)) << 4))
        Frag2x4 fa, fb;
        BAR_ONLY();
        K1_READ(fa, K1_AA(0), 0);
        K1_WAIT(fa);
#pragma unroll 1
        for (int s = 0; s < 18; s += 2) {
            BAR_ONLY();
            K1_STEP(fa, fb, K1_AA(s + 1), ((s + 1) & 3) * K1_STG);
            K1_WAIT(fb);
            BAR_ONLY();
            K1_STEP(fb, fa, K1_AA(s + 2), ((s + 2) & 3) * K1_STG);
            K1_WAIT(fa);
        }
        BAR_ONLY();
        K1_STEP(fa, fb, K1_AA(19), 3 * K1_STG);
        K1_WAIT(fb);
        K1_MMA_CT(fb, 0); K1_MMA_CT(fb, 1); K1_MMA_CT(fb, 2); K1_MMA_CT(fb, 3);
    }
    __syncthreads();
    STAMP(0, 2);

#pragma unroll
    for (int ct = 0; ct < 4; ++ct) {
        if (ct < nct) {
            int col = (wc * 4 + ct) * 16 + l15;
#pragma unroll
            for (int rt = 0; rt < 2; ++rt) {
                u32x2 v;
                v[0] = pack2(acc[rt][ct][0], acc[rt][ct][1]);
                v[1] = pack2(acc[rt][ct][2], acc[rt][ct][3]);
                *(u32x2*)(smem + K1_T + col * K1_TRS + (wr * 32 + rt * 16 + 4 * g) * 2) = v;
            }
        }
    }
    {
        int c = 240 + (tid >> 5), w = tid & 31;
        *(unsigned*)(smem + K1_T + c * K1_TRS + w * 4) = (c == 240) ? 0x3f803f80u : 0u;
    }
    __syncthreads();
    STAMP(0, 3);

    {
        const int q4 = l15 >> 2, p4 = lane & 3;
        const int rowt = (wave >> 1) * 16, sec = wave & 1;
        float p[4][5];
#pragma unroll
        for (int hh = 0; hh < 4; ++hh) {
            bf16x8 wp0 = *(const bf16x8*)(smem + K1_WP + ((hh * 2 + 0) * 64 + lane) * 16);
            bf16x8 wp1 = *(const bf16x8*)(smem + K1_WP + ((hh * 2 + 1) * 64 + lane) * 16);
            int c0 = sec * 80 + hh * 20 + 8 * g + q4;
            const char* tb = smem + K1_T + c0 * K1_TRS + (rowt + 4 * p4) * 2;
            bf16x8 b = cat8v(tr_read(tb), tr_read(tb + 4 * K1_TRS));
            f32x4 z = {0.f, 0.f, 0.f, 0.f};
            f32x4 d0 = MFMA(wp0, b, z);
            f32x4 d1 = MFMA(wp1, b, z);
            p[hh][0] = rbf(d0[0]); p[hh][1] = rbf(d0[1]); p[hh][2] = rbf(d0[2]); p[hh][3] = rbf(d0[3]); p[hh][4] = rbf(d1[0]);
        }
#pragma unroll
        for (int hh = 0; hh < 4; ++hh) {
            float mx = fmaxf(fmaxf(fmaxf(p[hh][0], p[hh][1]), fmaxf(p[hh][2], p[hh][3])), p[hh][4]);
            float mn = fminf(fminf(fminf(p[hh][0], p[hh][1]), fminf(p[hh][2], p[hh][3])), p[hh][4]);
            mx = max_g(mx);
            mn = min_g(mn);
            float ep[5], en[5], sp = 0.f, sn = 0.f;
#pragma unroll
            for (int i = 0; i < 5; ++i) {
                ep[i] = __expf(p[hh][i] - mx); sp += ep[i];
                en[i] = __expf(mn - p[hh][i]); sn += en[i];
            }
            sp = sum_g(sp);
            sn = sum_g(sn);
            float ip = __builtin_amdgcn_rcpf(sp), in_ = __builtin_amdgcn_rcpf(sn);
            u32x4 o;
            o[0] = pack2(ep[0] * ip, ep[1] * ip);
            o[1] = pack2(ep[2] * ip, ep[3] * ip);
            o[2] = pack2(ep[4] * ip, en[0] * in_);
            o[3] = pack2(en[1] * in_, en[2] * in_);
            unsigned o4 = pack2(en[3] * in_, en[4] * in_);
            if (sec == 0) {
                char* dst = phiq + (size_t)(((row0 + rowt) >> 4) * H_ + hg * 4 + hh) * PHT_;
                *(u32x4*)(dst + lane * 16) = o;
                *(unsigned*)(dst + 1024 + l15 * 16 + g * 4) = o4;
            } else {
                char* dst = smem + K1_PK + (rowt + l15) * K1_PKRS + hh * 80;
                *(u32x4*)(dst + 16 * g) = o;
                *(unsigned*)(dst + 64 + 4 * g) = o4;
            }
        }
    }
    __syncthreads();
    STAMP(0, 4);

    {
        const int hh = wave >> 1, dt = wave & 1;
        const int q4 = l15 >> 2, p4 = lane & 3;
        f32x4 kacc[3];
#pragma unroll
        for (int ft = 0; ft < 3; ++ft) kacc[ft] = (f32x4){0.f, 0.f, 0.f, 0.f};
#pragma unroll
        for (int ks = 0; ks < 2; ++ks) {
            int c = 160 + hh * 20 + dt * 16 + l15;
            if (dt == 1 && l15 >= 4) c = 240;
            bf16x8 b = *(const bf16x8*)(smem + K1_T + c * K1_TRS + (32 * ks + 8 * g) * 2);
#pragma unroll
            for (int ft = 0; ft < 3; ++ft) {
                const char* pb = smem + K1_PK + (32 * ks + 8 * g + q4) * K1_PKRS + (hh * 40 + 16 * ft + 4 * p4) * 2;
                bf16x8 a = cat8v(tr_read(pb), tr_read(pb + 4 * K1_PKRS));
                kacc[ft] = MFMA(a, b, kacc[ft]);
            }
        }
        float* dst = kvp + (size_t)(rb * H_ + hg * 4 + hh) * 40 * 32;
#pragma unroll
        for (int ft = 0; ft < 3; ++ft)
#pragma unroll
            for (int j = 0; j < 4; ++j) {
                int f = 16 * ft + 4 * g + j;
                if (f < 40) dst[f * 32 + dt * 16 + l15] = kacc[ft][j];
            }
    }
    STAMP(0, 5);
}

__global__ __launch_bounds__(256) void kr_reduce(const float* __restrict__ kvp, u16* __restrict__ kvt,
                                                 const u16* __restrict__ wg, const u16* __restrict__ wu,
                                                 float* __restrict__ csum) {
    __shared__ float red[2048];
    const int tid = threadIdx.x;
    const int bid = blockIdx.x;
    if (bid < 1280) {
        const int d = tid & 31, part = tid >> 5;
        const int h = bid / 40, f = bid - h * 40;
        float s = 0.f;
#pragma unroll
        for (int i = 0; i < 4; ++i) s += kvp[((size_t)((part * 4 + i) * H_ + h) * 40 + f) * 32 + d];
        red[tid] = s;
        __syncthreads();
        if (tid < 21) {
            float t = 0.f;
#pragma unroll
            for (int i = 0; i < 8; ++i) t += red[i * 32 + tid];
            kvt[h * KVT_H + tid * 40 + f] = f2bf(t);
        }
    } else {
        const int cbk = bid - 1280;
        const u16* w = (cbk < 27) ? wg : wu;
        const int col0 = (cbk % 27) * 64;
        const int c8 = tid & 7, rg = tid >> 3;
        float s[8] = {0.f, 0.f, 0.f, 0.f, 0.f, 0.f, 0.f, 0.f};
        u32x4 v[20];
#pragma unroll
        for (int i = 0; i < 20; ++i) v[i] = *(const u32x4*)(w + (size_t)(rg * 20 + i) * DF_ + col0 + c8 * 8);
#pragma unroll
        for (int i = 0; i < 20; ++i)
#pragma unroll
            for (int j = 0; j < 4; ++j) { s[2 * j] += lo16(v[i][j]); s[2 * j + 1] += hi16(v[i][j]); }
#pragma unroll
        for (int j = 0; j < 8; ++j) red[rg * 64 + c8 * 8 + j] = s[j];
        __syncthreads();
        if (tid < 64) {
            float t = 0.f;
#pragma unroll
            for (int i = 0; i < 32; ++i) t += red[i * 64 + tid];
            csum[(cbk < 27 ? 0 : DF_) + col0 + tid] = t;
        }
    }
}

struct Frag2x5 { bf16x8 a[2]; bf16x4 bl[5], bh[5]; };
#define G5_READ_CT(N, ct) do { LDS_TR(N.bl[ct], l_, ct * 32); LDS_TR(N.bh[ct], h_, ct * 32); } while (0)
#define G5_MMA_CT(C, ct)                                                                   \
    do {                                                                                   \
        bf16x8 b_ = cat8v(C.bl[ct], C.bh[ct]);                                             \
        acc[0][ct] = MFMA(b_, C.a[0], acc[0][ct]);                                         \
        acc[1][ct] = MFMA(b_, C.a[1], acc[1][ct]);                                         \
    } while (0)
#define G5_READ(N, AADDR, AOFF, BLO, BHI)                                                  \
    do {                                                                                   \
        const unsigned a_ = (AADDR), l_ = (BLO), h_ = (BHI);                               \
        LDS_B128(N.a[0], a_, 0); LDS_B128(N.a[1], a_, AOFF);                               \
        G5_READ_CT(N, 0); G5_READ_CT(N, 1); G5_READ_CT(N, 2); G5_READ_CT(N, 3); G5_READ_CT(N, 4); \
    } while (0)
#define G5_STEP(C, N, AADDR, AOFF, BLO, BHI)                                               \
    do {                                                                                   \
        const unsigned a_ = (AADDR), l_ = (BLO), h_ = (BHI);                               \
        LDS_B128(N.a[0], a_, 0); G5_READ_CT(N, 0); SB0(); G5_MMA_CT(C, 0); SB0();          \
        LDS_B128(N.a[1], a_, AOFF); G5_READ_CT(N, 1); SB0(); G5_MMA_CT(C, 1); SB0();       \
        G5_READ_CT(N, 2); SB0(); G5_MMA_CT(C, 2); SB0();                                   \
        G5_READ_CT(N, 3); SB0(); G5_MMA_CT(C, 3); SB0();                                   \
        G5_READ_CT(N, 4); SB0(); G5_MMA_CT(C, 4); SB0();                                   \
    } while (0)
#define G5_MMA(C) do { G5_MMA_CT(C, 0); G5_MMA_CT(C, 1); G5_MMA_CT(C, 2); G5_MMA_CT(C, 3); G5_MMA_CT(C, 4); } while (0)
#define G5_WAIT(F)                                                                                         \
    asm volatile("s_waitcnt lgkmcnt(0)"                                                                    \
                 : "+v"(F.a[0]), "+v"(F.a[1]), "+v"(F.bl[0]), "+v"(F.bl[1]), "+v"(F.bl[2]), "+v"(F.bl[3]),  \
                   "+v"(F.bl[4]), "+v"(F.bh[0]), "+v"(F.bh[1]), "+v"(F.bh[2]), "+v"(F.bh[3]), "+v"(F.bh[4]))

__device__ __forceinline__ void slice_stats(const float (&v)[20], float2* dst, int g) {
    float s = 0.f;
#pragma unroll
    for (int i = 0; i < 20; ++i) s += v[i];
    s = sum_g(s);
    float m = s * (1.0f / 80.0f);
    float q = 0.f;
#pragma unroll
    for (int i = 0; i < 20; ++i) { float t = v[i] - m; q += t * t; }
    q = sum_g(q);
    if (g == 0) *dst = make_float2(m, q);
}

constexpr int K2_O = 0;
constexpr int K2_B = 81920;
constexpr int K2_STG = 20480;
constexpr int K2_SMEM = K2_B + 3 * K2_STG;

__global__ __launch_bounds__(768, 3) void k2_attn_out(const u16* __restrict__ x, const char* __restrict__ phiq,
                                                      const u16* __restrict__ kvt, const u16* __restrict__ wo,
                                                      u16* __restrict__ xout, float2* __restrict__ stats) {
    extern __shared__ __attribute__((aligned(16))) char smem[];
    const int tid = threadIdx.x, lane = tid & 63, wave = __builtin_amdgcn_readfirstlane(tid >> 6);
    const int g = lane >> 4, l15 = lane & 15;
    const int rb = (blockIdx.x & 7) * 4 + ((blockIdx.x >> 3) & 3), cb = blockIdx.x >> 5;
    const int row0 = rb * 64, col0 = cb * 80;
    STAMP(1, 0);

    if (wave >= 8) {
        const int lw = wave - 8;
        const u16* src[5];
#pragma unroll
        for (int i = 0; i < 5; ++i) {
            int k, nch;
            wsrc<10>(lw + 4 * i, lane, k, nch);
            src[i] = wo + (size_t)k * D_ + col0 + nch * 8;
        }
        auto stage = [&](int s) {
            char* base = smem + K2_B + (s % 3) * K2_STG;
#pragma unroll
            for (int i = 0; i < 5; ++i) glds16(src[i] + (size_t)s * 128 * D_, base + (lw + 4 * i) * 1024);
        };
        stage(0); stage(1); stage(2);
#pragma unroll 1
        for (int b = 0; b < 5; ++b) {
            wait_stages<5, 1>(4 - b);
            BAR_ONLY();
            if (b >= 1 && b + 2 < 5) stage(b + 2);
        }
        return;
    }

#pragma unroll
    for (int bt = 0; bt < 2; ++bt) {
        bf16x8 a[2][2][2], b0[2][4], b1[2][4];
#pragma unroll
        for (int hi = 0; hi < 2; ++hi) {
            int h = wave * 4 + bt * 2 + hi;
#pragma unroll
            for (int rt = 0; rt < 4; ++rt) {
                const char* pr = phiq + (size_t)((rb * 4 + rt) * H_ + h) * PHT_;
                b0[hi][rt] = *(const bf16x8*)(pr + lane * 16);
                b1[hi][rt] = *(const bf16x8*)(pr + 1024 + l15 * 16);
            }
#pragma unroll
            for (int dt = 0; dt < 2; ++dt) {
                int d = 16 * dt + l15; if (d > 20) d = 20;
                a[hi][dt][0] = *(const bf16x8*)(kvt + h * KVT_H + d * 40 + 8 * g);
                a[hi][dt][1] = *(const bf16x8*)(kvt + h * KVT_H + d * 40 + 32);
            }
        }
#pragma unroll
        for (int hi = 0; hi < 2; ++hi) {
            int h = wave * 4 + bt * 2 + hi;
#pragma unroll
            for (int rt = 0; rt < 4; ++rt) {
                int r = rt * 16 + l15;
                bf16x8 bb1 = b1[hi][rt];
                if (g != 0) bb1 = (bf16x8){0, 0, 0, 0, 0, 0, 0, 0};
                f32x4 z = {0.f, 0.f, 0.f, 0.f};
                f32x4 d0 = MFMA(a[hi][0][0], b0[hi][rt], z); d0 = MFMA(a[hi][0][1], bb1, d0);
                f32x4 d1 = MFMA(a[hi][1][0], b0[hi][rt], z); d1 = MFMA(a[hi][1][1], bb1, d1);
                float dsel = (g == 1) ? d1[0] : 0.f;
                float den = sum_g(dsel);
                den = rbf(rbf(den) + 1e-6f);
                float rd = 1.0f / den;
                u32x2 v;
                v[0] = pack2(rbf(d0[0]) * rd, rbf(d0[1]) * rd);
                v[1] = pack2(rbf(d0[2]) * rd, rbf(d0[3]) * rd);
                int bo = (h * 20 + 4 * g) * 2;
                *(u32x2*)(smem + K2_O + r * 1280 + (((bo >> 4) ^ (r & 15)) << 4) + (bo & 15)) = v;
                if (g == 0) {
                    v[0] = pack2(rbf(d1[0]) * rd, rbf(d1[1]) * rd);
                    v[1] = pack2(rbf(d1[2]) * rd, rbf(d1[3]) * rd);
                    bo = (h * 20 + 16) * 2;
                    *(u32x2*)(smem + K2_O + r * 1280 + (((bo >> 4) ^ (r & 15)) << 4) + (bo & 15)) = v;
                }
            }
        }
    }
    asm volatile("s_waitcnt lgkmcnt(0)" ::: "memory");
    STAMP(1, 1);

    const int wr = wave & 1, kq = wave >> 1;
    f32x4 acc[2][5];
#pragma unroll
    for (int i = 0; i < 2; ++i)
#pragma unroll
        for (int j = 0; j < 5; ++j) acc[i][j] = (f32x4){0.f, 0.f, 0.f, 0.f};
    {
        const unsigned sm = lds_addr(smem);
        unsigned blo, bhi;
        wfrag_addr<160>(sm + K2_B + kq * 5120, lane, blo, bhi);
        const unsigned aad = sm + K2_O + (wr * 32 + l15) * 1280;
#define K2_AA(s) (aad + (((unsigned)(((s) * 16 + kq * 4 + g) ^ l15)) << 4))
        Frag2x5 fa, fb;
        BAR_ONLY();
        G5_READ(fa, K2_AA(0), 16 * 1280, blo, bhi);
        G5_WAIT(fa);
        BAR_ONLY();
        G5_STEP(fa, fb, K2_AA(1), 16 * 1280, blo + K2_STG, bhi + K2_STG);
        G5_WAIT(fb);
        BAR_ONLY();
        G5_STEP(fb, fa, K2_AA(2), 16 * 1280, blo + 2 * K2_STG, bhi + 2 * K2_STG);
        G5_WAIT(fa);
        BAR_ONLY();
        G5_STEP(fa, fb, K2_AA(3), 16 * 1280, blo, bhi);
        G5_WAIT(fb);
        BAR_ONLY();
        G5_STEP(fb, fa, K2_AA(4), 16 * 1280, blo + K2_STG, bhi + K2_STG);
        G5_WAIT(fa);
        G5_MMA(fa);
    }
    __syncthreads();
    float* red = (float*)smem;
    if (kq > 0) {
#pragma unroll
        for (int rt = 0; rt < 2; ++rt)
#pragma unroll
            for (int ct = 0; ct < 5; ++ct) *(f32x4*)(red + ((((kq - 1) * 4 + wr * 2 + rt) * 5 + ct) * 64 + lane) * 4) = acc[rt][ct];
    }
    __syncthreads();
    STAMP(1, 2);
    if (kq < 2) {
        const int rt = kq;
        f32x4 o[5];
        if (kq == 0) {
#pragma unroll
            for (int ct = 0; ct < 5; ++ct) o[ct] = acc[0][ct];
        } else {
#pragma unroll
            for (int ct = 0; ct < 5; ++ct) o[ct] = acc[1][ct];
        }
        (void)rt; (void)o;
    }
    if (kq == 0) {
#pragma unroll
        for (int rt = 0; rt < 2; ++rt) {
            int row = row0 + wr * 32 + rt * 16 + l15;
            float vals[20];
#pragma unroll
            for (int ct = 0; ct < 5; ++ct) {
                f32x4 o = acc[rt][ct];
#pragma unroll
                for (int q = 0; q < 3; ++q) o += *(const f32x4*)(red + (((q * 4 + wr * 2 + rt) * 5 + ct) * 64 + lane) * 4);
                int col = col0 + ct * 16 + 4 * g;
                u32x2 xv = *(const u32x2*)(x + (size_t)row * D_ + col);
                vals[ct * 4 + 0] = rbf(lo16(xv[0]) + rbf(o[0]));
                vals[ct * 4 + 1] = rbf(hi16(xv[0]) + rbf(o[1]));
                vals[ct * 4 + 2] = rbf(lo16(xv[1]) + rbf(o[2]));
                vals[ct * 4 + 3] = rbf(hi16(xv[1]) + rbf(o[3]));
                u32x2 v;
                v[0] = pack2(vals[ct * 4 + 0], vals[ct * 4 + 1]);
                v[1] = pack2(vals[ct * 4 + 2], vals[ct * 4 + 3]);
                *(u32x2*)(xout + (size_t)row * D_ + col) = v;
            }
            slice_stats(vals, stats + (size_t)row * 8 + cb, g);
        }
    }
    STAMP(1, 3);
}

constexpr int K3_STG = 49152;
constexpr int K3_ST = 3 * K3_STG;
constexpr int K3_SMEM = K3_ST + 2048;

__device__ __forceinline__ float gelu_tanh(float x) {
    float y2 = 1.5957691216057308f * (x + 0.044715f * x * x * x);
    float e = __expf(y2);
    return x - x * __builtin_amdgcn_rcpf(1.0f + e);
}

struct Frag4x4 { bf16x8 a[4]; bf16x4 bl[4], bh[4]; };
#define F4_STEP(C, N, AADDR, SBO)                                                          \
    do {                                                                                   \
        const unsigned a_ = (AADDR), sb_ = (SBO);                                          \
        _Pragma("unroll") for (int ct = 0; ct < 4; ++ct) {                                 \
            LDS_TR(N.bl[ct], blo3[ct] + sb_, 0);  LDS_TR(N.bh[ct], blo3[ct] + sb_, 1024);  \
            if (ct == 0) LDS_B128(N.a[0], a_, 0);                                          \
            if (ct == 1) LDS_B128(N.a[1], a_, 2048);                                       \
            if (ct == 2) LDS_B128(N.a[2], a_, 4096);                                       \
            if (ct == 3) LDS_B128(N.a[3], a_, 6144);                                       \
            SB0();                                                                         \
            bf16x8 b_ = cat8v(C.bl[ct], C.bh[ct]);                                         \
            _Pragma("unroll") for (int rt = 0; rt < 4; ++rt)                               \
                acc[rt][ct] = MFMA(b_, C.a[rt], acc[rt][ct]);                              \
            SB0();                                                                         \
        }                                                                                  \
    } while (0)
#define F4_READ(N, AADDR, SBO)                                                             \
    do {                                                                                   \
        const unsigned a_ = (AADDR), sb_ = (SBO);                                          \
        _Pragma("unroll") for (int ct = 0; ct < 4; ++ct) {                                 \
            LDS_TR(N.bl[ct], blo3[ct] + sb_, 0);  LDS_TR(N.bh[ct], blo3[ct] + sb_, 1024);  \
        }                                                                                  \
        LDS_B128(N.a[0], a_, 0); LDS_B128(N.a[1], a_, 2048); LDS_B128(N.a[2], a_, 4096); LDS_B128(N.a[3], a_, 6144); \
    } while (0)
#define F4_MMA(C)                                                                          \
    do {                                                                                   \
        _Pragma("unroll") for (int ct = 0; ct < 4; ++ct) {                                 \
            bf16x8 b_ = cat8v(C.bl[ct], C.bh[ct]);                                         \
            _Pragma("unroll") for (int rt = 0; rt < 4; ++rt)                               \
                acc[rt][ct] = MFMA(b_, C.a[rt], acc[rt][ct]);                              \
        }                                                                                  \
    } while (0)
#define F4_WAIT(F)                                                                                            \
    asm volatile("s_waitcnt lgkmcnt(0)"                                                                       \
                 : "+v"(F.a[0]), "+v"(F.a[1]), "+v"(F.a[2]), "+v"(F.a[3]), "+v"(F.bl[0]), "+v"(F.bl[1]),       \
                   "+v"(F.bl[2]), "+v"(F.bl[3]), "+v"(F.bh[0]), "+v"(F.bh[1]), "+v"(F.bh[2]), "+v"(F.bh[3]))

__global__ __launch_bounds__(768, 3) void k3_gate_up(const u16* __restrict__ x, const float2* __restrict__ pstats,
                                                     const u16* __restrict__ wg, const u16* __restrict__ wu,
                                                     const float* __restrict__ csum, u16* __restrict__ hbuf) {
    extern __shared__ __attribute__((aligned(16))) char smem[];
    const int tid = threadIdx.x, lane = tid & 63, wave = __builtin_amdgcn_readfirstlane(tid >> 6);
    const int g = lane >> 4, l15 = lane & 15;
    const int rb = blockIdx.x & 7, cb = blockIdx.x >> 3;
    const int row0 = rb * 256, col0 = cb * 64;
    float2* stats = (float2*)(smem + K3_ST);
    STAMP(2, 0);
    if (wave >= 8) {
        const int lw = wave - 8;
        const u16* srcA[8]; const u16* srcB[4];
#pragma unroll
        for (int i = 0; i < 8; ++i) {
            int c = (lw * 8 + i) * 64 + lane;
            int r = c >> 3, ch = (c & 7) ^ (r & 7);
            srcA[i] = x + (size_t)(row0 + r) * D_ + ch * 8;
        }
#pragma unroll
        for (int i = 0; i < 4; ++i) {
            int c = (lw * 4 + i) * 64 + lane;
            int k = c >> 4, cpos = c & 15;
            int sx = (k & 3) | (((k >> 3) & 1) << 2);
            int nch = (((cpos >> 1) ^ sx) << 1) | (cpos & 1);
            const u16* w = (nch < 8) ? wg : wu;
            srcB[i] = w + (size_t)k * DF_ + col0 + (nch & 7) * 8;
        }
        auto stage = [&](int s) {
            char* base = smem + (s % 3) * K3_STG;
#pragma unroll
            for (int i = 0; i < 4; ++i) glds16(srcB[i] + (size_t)s * 64 * DF_, base + 32768 + (lw * 4 + i) * 1024);
#pragma unroll
            for (int i = 0; i < 8; ++i) glds16(srcA[i] + s * 64, base + (lw * 8 + i) * 1024);
        };
        stage(0); stage(1);
#pragma unroll 1
        for (int b = 0; b < 10; ++b) {
            wait_stages<12, 1>(9 - b);
            BAR_ONLY();
            if (b + 2 < 10) stage(b + 2);
        }
        return;
    }
    if (tid < 256) {
        const f32x4* ps = (const f32x4*)(pstats + (size_t)(row0 + tid) * 8);
        f32x4 p0 = ps[0], p1 = ps[1], p2 = ps[2], p3 = ps[3];
        float mean = (p0[0] + p0[2] + p1[0] + p1[2] + p2[0] + p2[2] + p3[0] + p3[2]) * 0.125f;
        float m2 = p0[1] + p0[3] + p1[1] + p1[3] + p2[1] + p2[3] + p3[1] + p3[3];
        float dm = 0.f, t;
        t = p0[0] - mean; dm += t * t; t = p0[2] - mean; dm += t * t;
        t = p1[0] - mean; dm += t * t; t = p1[2] - mean; dm += t * t;
        t = p2[0] - mean; dm += t * t; t = p2[2] - mean; dm += t * t;
        t = p3[0] - mean; dm += t * t; t = p3[2] - mean; dm += t * t;
        float var_bf = rbf((m2 + 80.0f * dm) * (1.0f / 640.0f));
        float den = rbf(sqrtf(rbf(var_bf + 1e-5f)));
        stats[tid] = make_float2(rbf(mean), 1.0f / den);
    }
    asm volatile("s_waitcnt lgkmcnt(0)" ::: "memory");
    const int wr = wave >> 1, wc = wave & 1;
    f32x4 acc[4][4];
#pragma unroll
    for (int i = 0; i < 4; ++i)
#pragma unroll
        for (int j = 0; j < 4; ++j) acc[i][j] = (f32x4){0.f, 0.f, 0.f, 0.f};
    const unsigned sm3 = lds_addr(smem);
    unsigned blo3[4];
    {
        int q = l15 >> 2, p4 = lane & 3;
        int sx = q | ((g & 1) << 2);
#pragma unroll
        for (int ct = 0; ct < 4; ++ct) {
            int nt = (ct >> 1) * 4 + wc * 2 + (ct & 1);
            blo3[ct] = sm3 + 32768 + (8 * g + q) * 256 + ((nt ^ sx) * 32) + p4 * 8;
        }
    }
    unsigned aad3[2];
    {
        int r = wr * 64 + l15;
        aad3[0] = sm3 + r * 128 + (((unsigned)(g ^ (r & 7))) << 4);
        aad3[1] = sm3 + r * 128 + (((unsigned)((4 + g) ^ (r & 7))) << 4);
    }
    Frag4x4 fa, fb;
    BAR_ONLY();
    STAMP(2, 1);
    F4_READ(fa, aad3[0], 0);
    F4_WAIT(fa);
#pragma unroll 1
    for (int s = 0; s < 9; ++s) {
        const unsigned so = (s % 3) * K3_STG;
        F4_STEP(fa, fb, aad3[1] + so, so + 8192);
        F4_WAIT(fb);
        BAR_ONLY();
        const unsigned sn = ((s + 1) % 3) * K3_STG;
        F4_STEP(fb, fa, aad3[0] + sn, sn);
        F4_WAIT(fa);
    }
    F4_STEP(fa, fb, aad3[1], 8192);
    F4_WAIT(fb);
    F4_MMA(fb);
    STAMP(2, 2);
    {
        f32x4 cs[4];
#pragma unroll
        for (int c2 = 0; c2 < 2; ++c2) {
            int col = col0 + (wc * 2 + c2) * 16 + 4 * g;
            cs[c2] = *(const f32x4*)(csum + col);
            cs[2 + c2] = *(const f32x4*)(csum + DF_ + col);
        }
#pragma unroll
        for (int rt = 0; rt < 4; ++rt) {
            int rl = wr * 64 + rt * 16 + l15;
            float2 st = stats[rl];
#pragma unroll
            for (int c2 = 0; c2 < 2; ++c2) {
                int col = col0 + (wc * 2 + c2) * 16 + 4 * g;
                float hv[4];
#pragma unroll
                for (int j = 0; j < 4; ++j) {
                    float gg = rbf((acc[rt][c2][j] - st.x * cs[c2][j]) * st.y);
                    float uu = rbf((acc[rt][2 + c2][j] - st.x * cs[2 + c2][j]) * st.y);
                    hv[j] = rbf(gelu_tanh(gg)) * uu;
                }
                u32x2 v;
                v[0] = pack2(hv[0], hv[1]);
                v[1] = pack2(hv[2], hv[3]);
                *(u32x2*)(hbuf + (size_t)(row0 + rl) * DF_ + col) = v;
            }
        }
    }
    STAMP(2, 3);
}

constexpr int K4_STG = 36864;
constexpr int K4_SMEM = 4 * K4_STG;

__global__ __launch_bounds__(768, 3) void k4_down(const u16* __restrict__ hbuf, const u16* __restrict__ wd,
                                                  const u16* __restrict__ xin, u16* __restrict__ xout) {
    extern __shared__ __attribute__((aligned(16))) char smem[];
    const int tid = threadIdx.x, lane = tid & 63, wave = __builtin_amdgcn_readfirstlane(tid >> 6);
    const int g = lane >> 4, l15 = lane & 15;
    const int rb = (blockIdx.x & 7) * 4 + ((blockIdx.x >> 3) & 3), cb = blockIdx.x >> 5;
    const int row0 = rb * 64, col0 = cb * 80;
    constexpr int NS = 14;
    STAMP(3, 0);

    if (wave >= 8) {
        const int lw = wave - 8;
        const u16* src[9]; int adv[9];
#pragma unroll
        for (int i = 0; i < 9; ++i) {
            int ins = lw + 4 * i;
            if (ins < 16) {
                int t = ins >> 3, c = (ins & 7) * 64 + lane;
                int r = c >> 3, ch = (c & 7) ^ (r & 7);
                src[i] = hbuf + (size_t)(row0 + r) * DF_ + t * 64 + ch * 8; adv[i] = 128;
            } else {
                int k, nch;
                wsrc<10>(ins - 16, lane, k, nch);
                src[i] = wd + (size_t)k * D_ + col0 + nch * 8; adv[i] = 128 * D_;
            }
        }
        auto stage = [&](int s) {
            char* base = smem + (s & 3) * K4_STG;
#pragma unroll
            for (int i = 0; i < 9; ++i) {
                int ins = lw + 4 * i;
                bool upper = (ins >= 8 && ins < 16) || ins >= 26;
                int ss = (s == NS - 1 && upper) ? s - 1 : s;
                glds16(src[i] + (size_t)ss * adv[i], base + ins * 1024);
            }
        };
        stage(0); stage(1); stage(2);
#pragma unroll 1
        for (int b = 0; b < NS; ++b) {
            wait_stages<9, 2>(NS - 1 - b);
            BAR_ONLY();
            if (b + 3 < NS) stage(b + 3);
        }
        return;
    }

    const int wr = wave & 1, kq = wave >> 1;
    f32x4 acc[2][5];
#pragma unroll
    for (int i = 0; i < 2; ++i)
#pragma unroll
        for (int j = 0; j < 5; ++j) acc[i][j] = (f32x4){0.f, 0.f, 0.f, 0.f};
    {
        const unsigned sm = lds_addr(smem);
        unsigned blo, bhi;
        wfrag_addr<160>(sm + 16384 + kq * 5120, lane, blo, bhi);
        const int r = wr * 32 + l15;
        const unsigned aad = sm + (kq >> 1) * 8192 + r * 128 + (((unsigned)(((kq & 1) * 4 + g) ^ (r & 7))) << 4);
        Frag2x5 fa, fb;
        BAR_ONLY();
        G5_READ(fa, aad, 2048, blo, bhi);
        G5_WAIT(fa);
#pragma unroll 1
        for (int s = 0; s < NS - 2; s += 2) {
            BAR_ONLY();
            { const unsigned so = ((s + 1) & 3) * K4_STG; G5_STEP(fa, fb, aad + so, 2048, blo + so, bhi + so); }
            G5_WAIT(fb);
            BAR_ONLY();
            { const unsigned so = ((s + 2) & 3) * K4_STG; G5_STEP(fb, fa, aad + so, 2048, blo + so, bhi + so); }
            G5_WAIT(fa);
        }
        BAR_ONLY();
        { const unsigned so = ((NS - 1) & 3) * K4_STG; G5_STEP(fa, fb, aad + so, 2048, blo + so, bhi + so); }
        G5_WAIT(fb);
        if (kq < 2) G5_MMA(fb);
    }
    __syncthreads();
    float* red = (float*)smem;
    if (kq > 0) {
#pragma unroll
        for (int rt = 0; rt < 2; ++rt)
#pragma unroll
            for (int ct = 0; ct < 5; ++ct) *(f32x4*)(red + ((((kq - 1) * 4 + wr * 2 + rt) * 5 + ct) * 64 + lane) * 4) = acc[rt][ct];
    }
    __syncthreads();
    STAMP(3, 1);
    if (kq == 0) {
#pragma unroll
        for (int rt = 0; rt < 2; ++rt) {
            int row = row0 + wr * 32 + rt * 16 + l15;
#pragma unroll
            for (int ct = 0; ct < 5; ++ct) {
                f32x4 o = acc[rt][ct];
#pragma unroll
                for (int q = 0; q < 3; ++q) o += *(const f32x4*)(red + (((q * 4 + wr * 2 + rt) * 5 + ct) * 64 + lane) * 4);
                int col = col0 + ct * 16 + 4 * g;
                u32x2 xv = *(const u32x2*)(xin + (size_t)row * D_ + col);
                u32x2 v;
                v[0] = pack2(lo16(xv[0]) + rbf(o[0]), hi16(xv[0]) + rbf(o[1]));
                v[1] = pack2(lo16(xv[1]) + rbf(o[2]), hi16(xv[1]) + rbf(o[3]));
                *(u32x2*)(xout + (size_t)row * D_ + col) = v;
            }
        }
    }
    STAMP(3, 2);
}

extern "C" void kernel_launch(void* const* d_in, const int* in_sizes, int n_in,
                              void* d_out, int out_size, void* d_ws, size_t ws_size,
                              hipStream_t stream) {
    const u16* A_x = (const u16*)d_in[0];
    const u16* wqkv = (const u16*)d_in[1];
    const u16* wphi = (const u16*)d_in[2];
    const u16* wo = (const u16*)d_in[3];
    const u16* wg = (const u16*)d_in[5];
    const u16* wu = (const u16*)d_in[6];
    const u16* wd = (const u16*)d_in[7];
    u16* out = (u16*)d_out;

    char* ws = (char*)d_ws;
    u16* x1 = (u16*)(ws);
    u16* x2 = (u16*)(ws + (4u << 20));
    char* phiq = ws + (8u << 20);
    float* kvp = (float*)(ws + (16u << 20));
    u16* kvt = (u16*)(ws + (24u << 20));
    float2* stats = (float2*)(ws + (25u << 20));
    float* csum = (float*)(ws + (26u << 20));
    u16* hbuf = (u16*)(ws + (28u << 20));

    (void)hipFuncSetAttribute((const void*)k1_qkv, hipFuncAttributeMaxDynamicSharedMemorySize, K1_SMEM);
    (void)hipFuncSetAttribute((const void*)k2_attn_out, hipFuncAttributeMaxDynamicSharedMemorySize, K2_SMEM);
    (void)hipFuncSetAttribute((const void*)k3_gate_up, hipFuncAttributeMaxDynamicSharedMemorySize, K3_SMEM);
    (void)hipFuncSetAttribute((const void*)k4_down, hipFuncAttributeMaxDynamicSharedMemorySize, K4_SMEM);

    const u16* xin = A_x;
    for (int layer = 0; layer < 2; ++layer) {
        u16* xmid = x1;
        u16* xo = (layer == 0) ? x2 : out;
        k1_qkv<<<256, 768, K1_SMEM, stream>>>(xin, wqkv, wphi, phiq, kvp);
        kr_reduce<<<layer == 0 ? 1280 + 54 : 1280, 256, 0, stream>>>(kvp, kvt, wg, wu, csum);
        k2_attn_out<<<256, 768, K2_SMEM, stream>>>(xin, phiq, kvt, wo, xmid, stats);
        k3_gate_up<<<216, 768, K3_SMEM, stream>>>(xmid, stats, wg, wu, csum, hbuf);
        k4_down<<<256, 768, K4_SMEM, stream>>>(hbuf, wd, xmid, xo);
        xin = x2;
    }
}
```

```cpp
#include <hip/hip_runtime.h>
#include <hip/hip_bf16.h>
#include <stdint.h>

#define STAMP(k, i)
constexpr int S_ = 2048, D_ = 640, H_ = 32, DH_ = 20, DF_ = 1728;
constexpr int N3_ = 3 * D_;

typedef __attribute__((ext_vector_type(8))) short bf16x8;
typedef __attribute__((ext_vector_type(4))) short bf16x4;
typedef __attribute__((ext_vector_type(4))) float f32x4;
typedef __attribute__((ext_vector_type(4))) unsigned int u32x4;
typedef __attribute__((ext_vector_type(2))) unsigned int u32x2;
typedef unsigned short u16;
typedef __attribute__((address_space(3))) bf16x4* lds_b4_ptr;
typedef __attribute__((address_space(3))) void* lds_vptr;
typedef const __attribute__((address_space(1))) void* glb_vptr;

__device__ __forceinline__ float bf2f(u16 v) { return __uint_as_float(((unsigned)v) << 16); }
__device__ __forceinline__ u16 f2bf(float f) { return __builtin_bit_cast(u16, (__bf16)f); }
__device__ __forceinline__ float rbf(float f) { return bf2f(f2bf(f)); }
__device__ __forceinline__ unsigned pack2(float a, float b) { return (unsigned)f2bf(a) | ((unsigned)f2bf(b) << 16); }
__device__ __forceinline__ float lo16(unsigned u) { return __uint_as_float(u << 16); }
__device__ __forceinline__ float hi16(unsigned u) { return __uint_as_float(u & 0xffff0000u); }
__device__ __forceinline__ bf16x4 tr_read(const char* p) { return __builtin_amdgcn_ds_read_tr16_b64_v4i16((lds_b4_ptr)p); }
__device__ __forceinline__ bf16x8 cat8v(bf16x4 a, bf16x4 b) { return __builtin_shufflevector(a, b, 0, 1, 2, 3, 4, 5, 6, 7); }
#define MFMA(a, b, c) __builtin_amdgcn_mfma_f32_16x16x32_bf16((a), (b), (c), 0, 0, 0)

template <int CTRL> __device__ __forceinline__ float dpp_mov(float x) {
    return __int_as_float(__builtin_amdgcn_update_dpp(0, __float_as_int(x), CTRL, 0xf, 0xf, false));
}
__device__ __forceinline__ float sum16(float x) {
    x += dpp_mov<0x128>(x); x += dpp_mov<0x124>(x); x += dpp_mov<0x122>(x); x += dpp_mov<0x121>(x);
    return x;
}
__device__ __forceinline__ void swap16(float x, float& a, float& b) {
    auto r = __builtin_amdgcn_permlane16_swap(__float_as_uint(x), __float_as_uint(x), false, false);
    a = __uint_as_float(r[0]); b = __uint_as_float(r[1]);
}
__device__ __forceinline__ void swap32(float x, float& a, float& b) {
    auto r = __builtin_amdgcn_permlane32_swap(__float_as_uint(x), __float_as_uint(x), false, false);
    a = __uint_as_float(r[0]); b = __uint_as_float(r[1]);
}
__device__ __forceinline__ float sum_g(float x) { float a, b; swap16(x, a, b); x = a + b; swap32(x, a, b); return a + b; }
__device__ __forceinline__ float max_g(float x) { float a, b; swap16(x, a, b); x = fmaxf(a, b); swap32(x, a, b); return fmaxf(a, b); }
__device__ __forceinline__ float min_g(float x) { float a, b; swap16(x, a, b); x = fminf(a, b); swap32(x, a, b); return fminf(a, b); }

__device__ __forceinline__ u32x4 ln8(u32x4 v, float mean_bf, float rinv) {
    u32x4 o;
#pragma unroll
    for (int i = 0; i < 4; ++i) {
        float a = rbf(lo16(v[i]) - mean_bf) * rinv;
        float b = rbf(hi16(v[i]) - mean_bf) * rinv;
        o[i] = pack2(a, b);
    }
    return o;
}
__device__ __forceinline__ void row_stats16(const u32x4 (&c)[5], float& mean_bf, float& rinv) {
    float s = 0.f;
#pragma unroll
    for (int i = 0; i < 5; ++i)
#pragma unroll
        for (int j = 0; j < 4; ++j) { s += lo16(c[i][j]); s += hi16(c[i][j]); }
    s = sum16(s);
    float mean = s * (1.0f / 640.0f);
    float q = 0.f;
#pragma unroll
    for (int i = 0; i < 5; ++i)
#pragma unroll
        for (int j = 0; j < 4; ++j) {
            float a = lo16(c[i][j]) - mean, b = hi16(c[i][j]) - mean;
            q += a * a; q += b * b;
        }
    q = sum16(q);
    float var_bf = rbf(q * (1.0f / 640.0f));
    float den = rbf(sqrtf(rbf(var_bf + 1e-5f)));
    mean_bf = rbf(mean);
    rinv = 1.0f / den;
}

#define RAW_BAR() do { asm volatile("s_waitcnt lgkmcnt(0)" ::: "memory"); __builtin_amdgcn_s_barrier(); asm volatile("" ::: "memory"); } while (0)
__device__ __forceinline__ void glds16(const void* src, char* lds_dst) {
    __builtin_amdgcn_global_load_lds((glb_vptr)src, (lds_vptr)lds_dst, 16, 0, 0);
}
template <int CPR>
__device__ __forceinline__ void wsrc(int ins, int lane, int& k, int& nch) {
    int c = ins * 64 + lane;
    int pos = c / CPR;
    nch = c - pos * CPR;
    k = pos ^ (((pos >> 3) & 1) << 2);
}
#define LDS_TR(dst, addr, off) asm volatile("ds_read_b64_tr_b16 %0, %1 offset:%2" : "=v"(dst) : "v"(addr), "i"(off))
#define LDS_B128(dst, addr, off) asm volatile("ds_read_b128 %0, %1 offset:%2" : "=v"(dst) : "v"(addr), "i"(off))
__device__ __forceinline__ unsigned lds_addr(const void* p) { return (unsigned)(size_t)p; }
template <int RS>
__device__ __forceinline__ void wfrag_addr(unsigned img, int lane, unsigned& lo, unsigned& hi) {
    int g = lane >> 4, q = (lane & 15) >> 2, p = lane & 3;
    int sw = (g & 1) * 4;
    lo = img + (8 * g + sw + q) * RS + p * 8;
    hi = img + (8 * g + (4 ^ sw) + q) * RS + p * 8;
}
template <int N> __device__ __forceinline__ void wait_vm() { asm volatile("s_waitcnt vmcnt(%0)" ::"n"(N) : "memory"); }
template <int PER, int MAXS>
__device__ __forceinline__ void wait_stages(int stages) {
    if (stages >= MAXS) { wait_vm<PER * MAXS>(); return; }
    if constexpr (MAXS > 0) wait_stages<PER, MAXS - 1>(stages);
}

#define BAR_ONLY() do { asm volatile("" ::: "memory"); __builtin_amdgcn_s_barrier(); asm volatile("" ::: "memory"); } while (0)
#define SB0() __builtin_amdgcn_sched_barrier(0)

constexpr int PHT_ = 1280;
constexpr int KVT_H = 21 * 40;

constexpr int WQ_CH = 8 * 10 * 1920;
constexpr int WO_CH = 8 * 5 * 1280;
constexpr int WGU_CH = 27 * 10 * 1024;
constexpr int WD_CH = 8 * 14 * 1280;
constexpr int K0_CH = WQ_CH + WO_CH + WGU_CH + WD_CH;
constexpr int K0_CPBLK = (K0_CH + 1023) / 1024;
constexpr int K0_CSBLK = 54 + 30;
constexpr int K0_XBLK = 128;

__device__ __forceinline__ const u16* k0_src(int cc, const u16* wqkv, const u16* wo, const u16* wg, const u16* wu,
                                             const u16* wd, bool& zero) {
    zero = false;
    if (cc < WQ_CH) {
        int blk = cc / 1920, r = cc - blk * 1920;
        int hg = blk / 10, st = blk - hg * 10;
        int pos = r / 30, nch = r - pos * 30;
        int k = pos ^ (((pos >> 3) & 1) << 2);
        int sec = nch / 10, w = nch - sec * 10;
        return wqkv + (size_t)(st * 64 + k) * N3_ + sec * D_ + hg * 80 + w * 8;
    }
    cc -= WQ_CH;
    if (cc < WO_CH) {
        int blk = cc / 1280, r = cc - blk * 1280;
        int cb = blk / 5, st = blk - cb * 5;
        int pos = r / 10, nch = r - pos * 10;
        int k = pos ^ (((pos >> 3) & 1) << 2);
        return wo + (size_t)(st * 128 + k) * D_ + cb * 80 + nch * 8;
    }
    cc -= WO_CH;
    if (cc < WGU_CH) {
        int blk = cc >> 10, r = cc & 1023;
        int cb = blk / 10, st = blk - cb * 10;
        int k = r >> 4, cpos = r & 15;
        int sx = (k & 3) | (((k >> 3) & 1) << 2);
        int nch = (((cpos >> 1) ^ sx) << 1) | (cpos & 1);
        const u16* w = (nch < 8) ? wg : wu;
        return w + (size_t)(st * 64 + k) * DF_ + cb * 64 + (nch & 7) * 8;
    }
    cc -= WGU_CH;
    int blk = cc / 1280, r = cc - blk * 1280;
    int cb = blk / 14, st = blk - cb * 14;
    int pos = r / 10, nch = r - pos * 10;
    int k = st * 128 + (pos ^ (((pos >> 3) & 1) << 2));
    zero = (k >= DF_);
    if (zero) k = 0;
    return wd + (size_t)k * D_ + cb * 80 + nch * 8;
}

__global__ __launch_bounds__(256) void k0_prep(const u16* __restrict__ x, const u16* __restrict__ wqkv,
                                               const u16* __restrict__ wo, const u16* __restrict__ wg,
                                               const u16* __restrict__ wu, const u16* __restrict__ wd,
                                               u32x4* __restrict__ wt, float* __restrict__ csum,
                                               u16* __restrict__ xtile, float2* __restrict__ stats) {
    __shared__ float red[2048];
    const int tid = threadIdx.x, bid = blockIdx.x;
    if (bid < K0_XBLK) {
        const int lane = tid & 63, g = lane >> 4, l15 = lane & 15;
        const int row = bid * 16 + (tid >> 6) * 4 + g;
        u32x4 c[5];
#pragma unroll
        for (int i = 0; i < 5; ++i) c[i] = *(const u32x4*)(x + (size_t)row * D_ + (l15 + 16 * i) * 8);
        float sm = 0.f;
#pragma unroll
        for (int i = 0; i < 5; ++i)
#pragma unroll
            for (int j = 0; j < 4; ++j) { sm += lo16(c[i][j]); sm += hi16(c[i][j]); }
        sm = sum16(sm);
        float mean = sm * (1.0f / 640.0f), q = 0.f;
#pragma unroll
        for (int i = 0; i < 5; ++i)
#pragma unroll
            for (int j = 0; j < 4; ++j) {
                float a = lo16(c[i][j]) - mean, b = hi16(c[i][j]) - mean;
                q += a * a; q += b * b;
            }
        q = sum16(q);
        if (l15 < 8) stats[(size_t)row * 8 + l15] = make_float2(mean, q * 0.125f);
        const int r = row & 63;
#pragma unroll
        for (int i = 0; i < 5; ++i) {
            int ch = l15 + 16 * i;
            *(u32x4*)(xtile + (size_t)((row >> 6) * 10 + (ch >> 3)) * 4096 + r * 64 + (((ch & 7) ^ (r & 7)) << 3)) = c[i];
        }
    } else if (bid < K0_XBLK + K0_CSBLK) {
        const int cbk = bid - K0_XBLK;
        const u16* w; int ld, col0, obase;
        if (cbk < 54) { w = (cbk < 27) ? wg : wu; ld = DF_; col0 = (cbk % 27) * 64; obase = (cbk < 27 ? 0 : DF_) + col0; }
        else { w = wqkv; ld = N3_; col0 = (cbk - 54) * 64; obase = 2 * DF_ + col0; }
        const int c8 = tid & 7, rg = tid >> 3;
        float s[8] = {0.f, 0.f, 0.f, 0.f, 0.f, 0.f, 0.f, 0.f};
        u32x4 v[20];
#pragma unroll
        for (int i = 0; i < 20; ++i) v[i] = *(const u32x4*)(w + (size_t)(rg * 20 + i) * ld + col0 + c8 * 8);
#pragma unroll
        for (int i = 0; i < 20; ++i)
#pragma unroll
            for (int j = 0; j < 4; ++j) { s[2 * j] += lo16(v[i][j]); s[2 * j + 1] += hi16(v[i][j]); }
#pragma unroll
        for (int j = 0; j < 8; ++j) red[rg * 64 + c8 * 8 + j] = s[j];
        __syncthreads();
        if (tid < 64) {
            float t = 0.f;
#pragma unroll
            for (int i = 0; i < 32; ++i) t += red[i * 64 + tid];
            csum[obase + tid] = t;
        }
    } else {
        const int c0 = (bid - K0_XBLK - K0_CSBLK) * 1024 + tid;
        u32x4 v[4];
        bool z[4];
#pragma unroll
        for (int i = 0; i < 4; ++i) {
            int c = c0 + 256 * i; if (c >= K0_CH) c = K0_CH - 1;
            v[i] = *(const u32x4*)k0_src(c, wqkv, wo, wg, wu, wd, z[i]);
        }
#pragma unroll
        for (int i = 0; i < 4; ++i) {
            int c = c0 + 256 * i;
            if (c < K0_CH) wt[c] = z[i] ? (u32x4){0u, 0u, 0u, 0u} : v[i];
        }
    }
}

__device__ __forceinline__ float2 combine_stats(const float2* ps8) {
    const f32x4* ps = (const f32x4*)ps8;
    f32x4 p0 = ps[0], p1 = ps[1], p2 = ps[2], p3 = ps[3];
    float mean = (p0[0] + p0[2] + p1[0] + p1[2] + p2[0] + p2[2] + p3[0] + p3[2]) * 0.125f;
    float m2 = p0[1] + p0[3] + p1[1] + p1[3] + p2[1] + p2[3] + p3[1] + p3[3];
    float dm = 0.f, t;
    t = p0[0] - mean; dm += t * t; t = p0[2] - mean; dm += t * t;
    t = p1[0] - mean; dm += t * t; t = p1[2] - mean; dm += t * t;
    t = p2[0] - mean; dm += t * t; t = p2[2] - mean; dm += t * t;
    t = p3[0] - mean; dm += t * t; t = p3[2] - mean; dm += t * t;
    float var_bf = rbf((m2 + 80.0f * dm) * (1.0f / 640.0f));
    float den = rbf(sqrtf(rbf(var_bf + 1e-5f)));
    return make_float2(rbf(mean), 1.0f / den);
}

constexpr int K1_STG = 38912;
constexpr int K1_WP = 65536;
constexpr int K1_ST = 4 * K1_STG;
constexpr int K1_SMEM = K1_ST + 512;
constexpr int K1_T = 0;
constexpr int K1_TRS = 144;
constexpr int K1_PK = 256 * K1_TRS;
constexpr int K1_PKRS = 336;

struct Frag2x4 { bf16x8 a[2]; bf16x4 bl[4], bh[4]; };
#define K1_READ_CT(N, ct, so) do { LDS_TR(N.bl[ct], blo + (so), ct * 32); LDS_TR(N.bh[ct], bhi + (so), ct * 32); } while (0)
#define K1_WAIT(F)                                                                                      \
    asm volatile("s_waitcnt lgkmcnt(0)"                                                                 \
                 : "+v"(F.a[0]), "+v"(F.a[1]), "+v"(F.bl[0]), "+v"(F.bl[1]), "+v"(F.bl[2]), "+v"(F.bl[3]), \
                   "+v"(F.bh[0]), "+v"(F.bh[1]), "+v"(F.bh[2]), "+v"(F.bh[3]))
#define K1_READ(N, AA, SO)                                                                              \
    do {                                                                                                \
        const unsigned aa_ = (AA), so_ = (SO);                                                          \
        LDS_B128(N.a[0], aa_, 0); LDS_B128(N.a[1], aa_, 2048);                                          \
        K1_READ_CT(N, 0, so_); K1_READ_CT(N, 1, so_); K1_READ_CT(N, 2, so_); K1_READ_CT(N, 3, so_);     \
    } while (0)
#define K1_MMA_CT(C, ct)                                                                                \
    do {                                                                                                \
        bf16x8 b_ = cat8v(C.bl[ct], C.bh[ct]);                                                          \
        acc[0][ct] = MFMA(C.a[0], b_, acc[0][ct]);                                                      \
        acc[1][ct] = MFMA(C.a[1], b_, acc[1][ct]);                                                      \
    } while (0)
#define K1_STEP(C, N, AA, SO)                                                                           \
    do {                                                                                                \
        const unsigned aa_ = (AA), so_ = (SO);                                                          \
        LDS_B128(N.a[0], aa_, 0); K1_READ_CT(N, 0, so_); SB0(); K1_MMA_CT(C, 0); SB0();                 \
        LDS_B128(N.a[1], aa_, 2048); K1_READ_CT(N, 1, so_); SB0(); K1_MMA_CT(C, 1); SB0();              \
        K1_READ_CT(N, 2, so_); SB0(); K1_MMA_CT(C, 2); SB0();                                           \
        K1_READ_CT(N, 3, so_); SB0(); K1_MMA_CT(C, 3); SB0();                                           \
    } while (0)

__global__ __launch_bounds__(768, 3) void k1_qkv(const u16* __restrict__ xt, const float2* __restrict__ pstats,
                                                 const u16* __restrict__ wqkv, const float* __restrict__ csum,
                                                 const u16* __restrict__ wphi, char* __restrict__ phiq,
                                                 float* __restrict__ kvp) {
    extern __shared__ __attribute__((aligned(16))) char smem[];
    const int tid = threadIdx.x, lane = tid & 63, wave = __builtin_amdgcn_readfirstlane(tid >> 6);
    const int g = lane >> 4, l15 = lane & 15;
    const int hg = blockIdx.x & 7, rb = blockIdx.x >> 3;
    const int row0 = rb * 64;
    float2* stats = (float2*)(smem + K1_ST);
    STAMP(0, 0);

    if (wave >= 8) {
        const int lw = wave - 8;
        const char* srcA = (const char*)xt + (size_t)rb * 10 * 8192 + lw * 2048 + lane * 16;
        const char* srcB = (const char*)wqkv + (size_t)hg * 10 * 30720 + lw * 1024 + lane * 16;
        auto stage = [&](int s) {
            char* base = smem + (s & 3) * K1_STG;
#pragma unroll
            for (int i = 0; i < 2; ++i) glds16(srcA + (size_t)s * 8192 + i * 1024, base + lw * 2048 + i * 1024);
#pragma unroll
            for (int i = 0; i < 7; ++i) glds16(srcB + (size_t)s * 30720 + i * 4096, base + 8192 + lw * 1024 + i * 4096);
            if (lw < 2) glds16(srcB + (size_t)s * 30720 + 7 * 4096, base + 8192 + lw * 1024 + 7 * 4096);
        };
        stage(0); stage(1); stage(2);
#pragma unroll 1
        for (int b = 0; b < 10; ++b) {
            if (lw < 2) wait_stages<10, 2>(9 - b); else wait_stages<9, 2>(9 - b);
            BAR_ONLY();
            if (b + 3 < 10) stage(b + 3);
        }
        return;
    }

    if (tid < 64) stats[tid] = combine_stats(pstats + (size_t)(row0 + tid) * 8);
    bf16x8 wpfrag;
    {
        int hh = tid >> 7, t = (tid >> 6) & 1;
        int wf = (t == 0) ? (5 * (l15 >> 2) + (l15 & 3)) : (((l15 & 3) == 0) ? (5 * (l15 >> 2) + 4) : -1);
        int fc = wf < 0 ? 0 : wf;
        u16 wpv[8];
#pragma unroll
        for (int j = 0; j < 8; ++j) {
            int d = 8 * g + j; if (d > 19) d = 19;
            wpv[j] = wphi[((hg * 4 + hh) * DH_ + d) * DH_ + fc];
        }
        bf16x8 v;
#pragma unroll
        for (int j = 0; j < 8; ++j) v[j] = (wf >= 0 && 8 * g + j < DH_) ? (short)wpv[j] : (short)0;
        wpfrag = v;
    }
    asm volatile("s_waitcnt lgkmcnt(0)" ::: "memory");

    const int wr = wave >> 2, wc = wave & 3;
    f32x4 acc[2][4];
#pragma unroll
    for (int i = 0; i < 2; ++i)
#pragma unroll
        for (int j = 0; j < 4; ++j) acc[i][j] = (f32x4){0.f, 0.f, 0.f, 0.f};
    const int nct = (wc == 3) ? 3 : 4;
    {
        const unsigned sm = lds_addr(smem);
        unsigned blo, bhi;
        wfrag_addr<480>(sm + 8192 + wc * 4 * 32, lane, blo, bhi);
        const int r = wr * 32 + l15;
        const unsigned aad0 = sm + r * 128 + (((unsigned)(g ^ (r & 7))) << 4);
        const unsigned aad1 = sm + r * 128 + (((unsigned)((4 + g) ^ (r & 7))) << 4);
        Frag2x4 fa, fb;
        BAR_ONLY();
        STAMP(0, 1);
        K1_READ(fa, aad0, 0);
        K1_WAIT(fa);
#pragma unroll 1
        for (int s = 0; s < 9; ++s) {
            const unsigned so = (s & 3) * K1_STG;
            K1_STEP(fa, fb, aad1 + so, so + 32 * 480);
            K1_WAIT(fb);
            BAR_ONLY();
            const unsigned sn = ((s + 1) & 3) * K1_STG;
            K1_STEP(fb, fa, aad0 + sn, sn);
            K1_WAIT(fa);
        }
        K1_STEP(fa, fb, aad1 + K1_STG, K1_STG + 32 * 480);
        K1_WAIT(fb);
        K1_MMA_CT(fb, 0); K1_MMA_CT(fb, 1); K1_MMA_CT(fb, 2); K1_MMA_CT(fb, 3);
    }
    __syncthreads();
    STAMP(0, 2);

    *(bf16x8*)(smem + K1_WP + tid * 16) = wpfrag;
    {
        float2 st[2][4];
#pragma unroll
        for (int rt = 0; rt < 2; ++rt)
#pragma unroll
            for (int j = 0; j < 4; ++j) st[rt][j] = stats[wr * 32 + rt * 16 + 4 * g + j];
#pragma unroll
        for (int ct = 0; ct < 4; ++ct) {
            if (ct < nct) {
                int col = (wc * 4 + ct) * 16 + l15;
                int sec = col / 80;
                float cs = csum[2 * DF_ + sec * D_ + hg * 80 + (col - sec * 80)];
#pragma unroll
                for (int rt = 0; rt < 2; ++rt) {
                    float y[4];
#pragma unroll
                    for (int j = 0; j < 4; ++j) y[j] = (acc[rt][ct][j] - st[rt][j].x * cs) * st[rt][j].y;
                    u32x2 v;
                    v[0] = pack2(y[0], y[1]);
                    v[1] = pack2(y[2], y[3]);
                    *(u32x2*)(smem + K1_T + col * K1_TRS + (wr * 32 + rt * 16 + 4 * g) * 2) = v;
                }
            }
        }
    }
    {
        int c = 240 + (tid >> 5), w = tid & 31;
        *(unsigned*)(smem + K1_T + c * K1_TRS + w * 4) = (c == 240) ? 0x3f803f80u : 0u;
    }
    __syncthreads();
    STAMP(0, 3);

    {
        const int q4 = l15 >> 2, p4 = lane & 3;
        const int rowt = (wave >> 1) * 16, sec = wave & 1;
        float p[4][5];
#pragma unroll
        for (int hh = 0; hh < 4; ++hh) {
            bf16x8 wp0 = *(const bf16x8*)(smem + K1_WP + ((hh * 2 + 0) * 64 + lane) * 16);
            bf16x8 wp1 = *(const bf16x8*)(smem + K1_WP + ((hh * 2 + 1) * 64 + lane) * 16);
            int c0 = sec * 80 + hh * 20 + 8 * g + q4;
            const char* tb = smem + K1_T + c0 * K1_TRS + (rowt + 4 * p4) * 2;
            bf16x8 b = cat8v(tr_read(tb), tr_read(tb + 4 * K1_TRS));
            f32x4 z = {0.f, 0.f, 0.f, 0.f};
            f32x4 d0 = MFMA(wp0, b, z);
            f32x4 d1 = MFMA(wp1, b, z);
            p[hh][0] = rbf(d0[0]); p[hh][1] = rbf(d0[1]); p[hh][2] = rbf(d0[2]); p[hh][3] = rbf(d0[3]); p[hh][4] = rbf(d1[0]);
        }
#pragma unroll
        for (int hh = 0; hh < 4; ++hh) {
            float mx = fmaxf(fmaxf(fmaxf(p[hh][0], p[hh][1]), fmaxf(p[hh][2], p[hh][3])), p[hh][4]);
            float mn = fminf(fminf(fminf(p[hh][0], p[hh][1]), fminf(p[hh][2], p[hh][3])), p[hh][4]);
            mx = max_g(mx);
            mn = min_g(mn);
            float ep[5], en[5], sp = 0.f, sn = 0.f;
#pragma unroll
            for (int i = 0; i < 5; ++i) {
                ep[i] = __expf(p[hh][i] - mx); sp += ep[i];
                en[i] = __expf(mn - p[hh][i]); sn += en[i];
            }
            sp = sum_g(sp);
            sn = sum_g(sn);
            float ip = __builtin_amdgcn_rcpf(sp), in_ = __builtin_amdgcn_rcpf(sn);
            u32x4 o;
            o[0] = pack2(ep[0] * ip, ep[1] * ip);
            o[1] = pack2(ep[2] * ip, ep[3] * ip);
            o[2] = pack2(ep[4] * ip, en[0] * in_);
            o[3] = pack2(en[1] * in_, en[2] * in_);
            unsigned o4 = pack2(en[3] * in_, en[4] * in_);
            if (sec == 0) {
                char* dst = phiq + (size_t)(((row0 + rowt) >> 4) * H_ + hg * 4 + hh) * PHT_;
                *(u32x4*)(dst + lane * 16) = o;
                *(unsigned*)(dst + 1024 + l15 * 16 + g * 4) = o4;
            } else {
                char* dst = smem + K1_PK + (rowt + l15) * K1_PKRS + hh * 80;
                *(u32x4*)(dst + 16 * g) = o;
                *(unsigned*)(dst + 64 + 4 * g) = o4;
            }
        }
    }
    __syncthreads();
    STAMP(0, 4);

    {
        const int hh = wave >> 1, dt = wave & 1;
        const int q4 = l15 >> 2, p4 = lane & 3;
        f32x4 kacc[3];
#pragma unroll
        for (int ft = 0; ft < 3; ++ft) kacc[ft] = (f32x4){0.f, 0.f, 0.f, 0.f};
#pragma unroll
        for (int ks = 0; ks < 2; ++ks) {
            int c = 160 + hh * 20 + dt * 16 + l15;
            if (dt == 1 && l15 >= 4) c = 240;
            bf16x8 b = *(const bf16x8*)(smem + K1_T + c * K1_TRS + (32 * ks + 8 * g) * 2);
#pragma unroll
            for (int ft = 0; ft < 3; ++ft) {
                const char* pb = smem + K1_PK + (32 * ks + 8 * g + q4) * K1_PKRS + (hh * 40 + 16 * ft + 4 * p4) * 2;
                bf16x8 a = cat8v(tr_read(pb), tr_read(pb + 4 * K1_PKRS));
                kacc[ft] = MFMA(a, b, kacc[ft]);
            }
        }
        float* dst = kvp + (size_t)(rb * H_ + hg * 4 + hh) * 40 * 32;
#pragma unroll
        for (int ft = 0; ft < 3; ++ft)
#pragma unroll
            for (int j = 0; j < 4; ++j) {
                int f = 16 * ft + 4 * g + j;
                if (f < 40) dst[f * 32 + dt * 16 + l15] = kacc[ft][j];
            }
    }
    STAMP(0, 5);
}

__global__ __launch_bounds__(256) void kr_reduce(const float* __restrict__ kvp, u16* __restrict__ kvt) {
    __shared__ float red[256];
    const int tid = threadIdx.x;
    const int bid = blockIdx.x;
    const int d = tid & 31, part = tid >> 5;
    const int h = bid / 40, f = bid - h * 40;
    float s = 0.f;
#pragma unroll
    for (int i = 0; i < 4; ++i) s += kvp[((size_t)((part * 4 + i) * H_ + h) * 40 + f) * 32 + d];
    red[tid] = s;
    __syncthreads();
    if (tid < 21) {
        float t = 0.f;
#pragma unroll
        for (int i = 0; i < 8; ++i) t += red[i * 32 + tid];
        kvt[h * KVT_H + tid * 40 + f] = f2bf(t);
    }
}

struct Frag2x5 { bf16x8 a[2]; bf16x4 bl[5], bh[5]; };
#define G5_READ_CT(N, ct) do { LDS_TR(N.bl[ct], l_, ct * 32); LDS_TR(N.bh[ct], h_, ct * 32); } while (0)
#define G5_MMA_CT(C, ct)                                                                   \
    do {                                                                                   \
        bf16x8 b_ = cat8v(C.bl[ct], C.bh[ct]);                                             \
        acc[0][ct] = MFMA(b_, C.a[0], acc[0][ct]);                                         \
        acc[1][ct] = MFMA(b_, C.a[1], acc[1][ct]);                                         \
    } while (0)
#define G5_READ(N, AADDR, AOFF, BLO, BHI)                                                  \
    do {                                                                                   \
        const unsigned a_ = (AADDR), l_ = (BLO), h_ = (BHI);                               \
        LDS_B128(N.a[0], a_, 0); LDS_B128(N.a[1], a_, AOFF);                               \
        G5_READ_CT(N, 0); G5_READ_CT(N, 1); G5_READ_CT(N, 2); G5_READ_CT(N, 3); G5_READ_CT(N, 4); \
    } while (0)
#define G5_STEP(C, N, AADDR, AOFF, BLO, BHI)                                               \
    do {                                                                                   \
        const unsigned a_ = (AADDR), l_ = (BLO), h_ = (BHI);                               \
        LDS_B128(N.a[0], a_, 0); G5_READ_CT(N, 0); SB0(); G5_MMA_CT(C, 0); SB0();          \
        LDS_B128(N.a[1], a_, AOFF); G5_READ_CT(N, 1); SB0(); G5_MMA_CT(C, 1); SB0();       \
        G5_READ_CT(N, 2); SB0(); G5_MMA_CT(C, 2); SB0();                                   \
        G5_READ_CT(N, 3); SB0(); G5_MMA_CT(C, 3); SB0();                                   \
        G5_READ_CT(N, 4); SB0(); G5_MMA_CT(C, 4); SB0();                                   \
    } while (0)
#define G5_MMA(C) do { G5_MMA_CT(C, 0); G5_MMA_CT(C, 1); G5_MMA_CT(C, 2); G5_MMA_CT(C, 3); G5_MMA_CT(C, 4); } while (0)
#define G5_WAIT(F)                                                                                         \
    asm volatile("s_waitcnt lgkmcnt(0)"                                                                    \
                 : "+v"(F.a[0]), "+v"(F.a[1]), "+v"(F.bl[0]), "+v"(F.bl[1]), "+v"(F.bl[2]), "+v"(F.bl[3]),  \
                   "+v"(F.bl[4]), "+v"(F.bh[0]), "+v"(F.bh[1]), "+v"(F.bh[2]), "+v"(F.bh[3]), "+v"(F.bh[4]))

__device__ __forceinline__ void slice_stats(const float (&v)[20], float2* dst, int g) {
    float s = 0.f;
#pragma unroll
    for (int i = 0; i < 20; ++i) s += v[i];
    s = sum_g(s);
    float m = s * (1.0f / 80.0f);
    float q = 0.f;
#pragma unroll
    for (int i = 0; i < 20; ++i) { float t = v[i] - m; q += t * t; }
    q = sum_g(q);
    if (g == 0) *dst = make_float2(m, q);
}

constexpr int K2_O = 0;
constexpr int K2_B = 81920;
constexpr int K2_STG = 20480;
constexpr int K2_SMEM = K2_B + 3 * K2_STG;

__global__ __launch_bounds__(768, 3) void k2_attn_out(const u16* __restrict__ x, const char* __restrict__ phiq,
                                                      const u16* __restrict__ kvt, const u16* __restrict__ wo,
                                                      u16* __restrict__ xout, u16* __restrict__ xtile,
                                                      float2* __restrict__ stats) {
    extern __shared__ __attribute__((aligned(16))) char smem[];
    const int tid = threadIdx.x, lane = tid & 63, wave = __builtin_amdgcn_readfirstlane(tid >> 6);
    const int g = lane >> 4, l15 = lane & 15;
    const int rb = (blockIdx.x & 7) * 4 + ((blockIdx.x >> 3) & 3), cb = blockIdx.x >> 5;
    const int row0 = rb * 64, col0 = cb * 80;
    STAMP(1, 0);

    if (wave >= 8) {
        const int lw = wave - 8;
        const char* src = (const char*)wo + (size_t)cb * 5 * K2_STG + lw * 1024 + lane * 16;
        auto stage = [&](int s) {
            char* base = smem + K2_B + (s % 3) * K2_STG + lw * 1024;
#pragma unroll
            for (int i = 0; i < 5; ++i) glds16(src + (size_t)s * K2_STG + i * 4096, base + i * 4096);
        };
        stage(0); stage(1); stage(2);
#pragma unroll 1
        for (int b = 0; b < 5; ++b) {
            wait_stages<5, 1>(4 - b);
            BAR_ONLY();
            if (b >= 1 && b + 2 < 5) stage(b + 2);
        }
        return;
    }

    u32x2 xres[5];
    if (wave < 4) {
#pragma unroll
        for (int ct = 0; ct < 5; ++ct) xres[ct] = *(const u32x2*)(x + (size_t)(row0 + wave * 16 + l15) * D_ + col0 + ct * 16 + 4 * g);
    }
    {
        struct HF { bf16x8 a[2][2], b0[4], b1[4]; };
        auto load_head = [&](HF& F, int h) {
#pragma unroll
            for (int rt = 0; rt < 4; ++rt) {
                const char* pr = phiq + (size_t)((rb * 4 + rt) * H_ + h) * PHT_;
                F.b0[rt] = *(const bf16x8*)(pr + lane * 16);
                F.b1[rt] = *(const bf16x8*)(pr + 1024 + l15 * 16);
            }
#pragma unroll
            for (int dt = 0; dt < 2; ++dt) {
                int d = 16 * dt + l15; if (d > 20) d = 20;
                F.a[dt][0] = *(const bf16x8*)(kvt + h * KVT_H + d * 40 + 8 * g);
                F.a[dt][1] = *(const bf16x8*)(kvt + h * KVT_H + d * 40 + 32);
            }
        };
        auto do_head = [&](const HF& F, int h) {
            f32x4 d0[4], d1[4];
            const f32x4 z = {0.f, 0.f, 0.f, 0.f};
#pragma unroll
            for (int rt = 0; rt < 4; ++rt) { d0[rt] = MFMA(F.a[0][0], F.b0[rt], z); d1[rt] = MFMA(F.a[1][0], F.b0[rt], z); }
#pragma unroll
            for (int rt = 0; rt < 4; ++rt) {
                bf16x8 bb1 = F.b1[rt];
                if (g != 0) bb1 = (bf16x8){0, 0, 0, 0, 0, 0, 0, 0};
                d0[rt] = MFMA(F.a[0][1], bb1, d0[rt]);
                d1[rt] = MFMA(F.a[1][1], bb1, d1[rt]);
            }
            float rd[4];
#pragma unroll
            for (int rt = 0; rt < 4; ++rt) rd[rt] = sum_g((g == 1) ? d1[rt][0] : 0.f);
#pragma unroll
            for (int rt = 0; rt < 4; ++rt) rd[rt] = __builtin_amdgcn_rcpf(rbf(rbf(rd[rt]) + 1e-6f));
            const int bo = (h * 20 + 4 * g) * 2, bo2 = (h * 20 + 16) * 2;
#pragma unroll
            for (int rt = 0; rt < 4; ++rt) {
                int r = rt * 16 + l15;
                u32x2 v;
                v[0] = pack2(rbf(d0[rt][0]) * rd[rt], rbf(d0[rt][1]) * rd[rt]);
                v[1] = pack2(rbf(d0[rt][2]) * rd[rt], rbf(d0[rt][3]) * rd[rt]);
                *(u32x2*)(smem + K2_O + r * 1280 + (((bo >> 4) ^ (r & 15)) << 4) + (bo & 15)) = v;
                if (g == 0) {
                    v[0] = pack2(rbf(d1[rt][0]) * rd[rt], rbf(d1[rt][1]) * rd[rt]);
                    v[1] = pack2(rbf(d1[rt][2]) * rd[rt], rbf(d1[rt][3]) * rd[rt]);
                    *(u32x2*)(smem + K2_O + r * 1280 + (((bo2 >> 4) ^ (r & 15)) << 4) + (bo2 & 15)) = v;
                }
            }
        };
        HF f0, f1;
        const int h0 = wave * 4;
        load_head(f0, h0);
        load_head(f1, h0 + 1);
        do_head(f0, h0);
        load_head(f0, h0 + 2);
        do_head(f1, h0 + 1);
        load_head(f1, h0 + 3);
        do_head(f0, h0 + 2);
        do_head(f1, h0 + 3);
    }
    asm volatile("s_waitcnt lgkmcnt(0)" ::: "memory");
    STAMP(1, 1);

    const int wr = wave & 1, kq = wave >> 1;
    f32x4 acc[2][5];
#pragma unroll
    for (int i = 0; i < 2; ++i)
#pragma unroll
        for (int j = 0; j < 5; ++j) acc[i][j] = (f32x4){0.f, 0.f, 0.f, 0.f};
    {
        const unsigned sm = lds_addr(smem);
        unsigned blo, bhi;
        wfrag_addr<160>(sm + K2_B + kq * 5120, lane, blo, bhi);
        const unsigned aad = sm + K2_O + (wr * 32 + l15) * 1280;
#define K2_AA(s) (aad + (((unsigned)(((s) * 16 + kq * 4 + g) ^ l15)) << 4))
        Frag2x5 fa, fb;
        BAR_ONLY();
        G5_READ(fa, K2_AA(0), 16 * 1280, blo, bhi);
        G5_WAIT(fa);
        BAR_ONLY();
        G5_STEP(fa, fb, K2_AA(1), 16 * 1280, blo + K2_STG, bhi + K2_STG);
        G5_WAIT(fb);
        BAR_ONLY();
        G5_STEP(fb, fa, K2_AA(2), 16 * 1280, blo + 2 * K2_STG, bhi + 2 * K2_STG);
        G5_WAIT(fa);
        BAR_ONLY();
        G5_STEP(fa, fb, K2_AA(3), 16 * 1280, blo, bhi);
        G5_WAIT(fb);
        BAR_ONLY();
        G5_STEP(fb, fa, K2_AA(4), 16 * 1280, blo + K2_STG, bhi + K2_STG);
        G5_WAIT(fa);
        G5_MMA(fa);
    }
    __syncthreads();
    float* red = (float*)smem;
#pragma unroll
    for (int rt = 0; rt < 2; ++rt)
#pragma unroll
        for (int ct = 0; ct < 5; ++ct) *(f32x4*)(red + (((kq * 4 + wr * 2 + rt) * 5 + ct) * 64 + lane) * 4) = acc[rt][ct];
    __syncthreads();
    STAMP(1, 2);
    if (wave < 4) {
        const int row = row0 + wave * 16 + l15;
        float vals[20];
#pragma unroll
        for (int ct = 0; ct < 5; ++ct) {
            f32x4 o = *(const f32x4*)(red + ((wave * 5 + ct) * 64 + lane) * 4);
#pragma unroll
            for (int q = 1; q < 4; ++q) o += *(const f32x4*)(red + (((q * 4 + wave) * 5 + ct) * 64 + lane) * 4);
            int col = col0 + ct * 16 + 4 * g;
            vals[ct * 4 + 0] = rbf(lo16(xres[ct][0]) + rbf(o[0]));
            vals[ct * 4 + 1] = rbf(hi16(xres[ct][0]) + rbf(o[1]));
            vals[ct * 4 + 2] = rbf(lo16(xres[ct][1]) + rbf(o[2]));
            vals[ct * 4 + 3] = rbf(hi16(xres[ct][1]) + rbf(o[3]));
            u32x2 v;
            v[0] = pack2(vals[ct * 4 + 0], vals[ct * 4 + 1]);
            v[1] = pack2(vals[ct * 4 + 2], vals[ct * 4 + 3]);
            *(u32x2*)(xout + (size_t)row * D_ + col) = v;
            int r = row & 63, cc = col & 63;
            *(u32x2*)(xtile + (size_t)(rb * 10 + (col >> 6)) * 4096 + r * 64 + (((cc >> 3) ^ (r & 7)) << 3) + (cc & 7)) = v;
        }
        slice_stats(vals, stats + (size_t)row * 8 + cb, g);
    }
    STAMP(1, 3);
}

constexpr int K3_STG = 49152;
constexpr int K3_ST = 3 * K3_STG;
constexpr int K3_SMEM = K3_ST + 2048;

__device__ __forceinline__ float gelu_tanh(float x) {
    float y2 = 1.5957691216057308f * (x + 0.044715f * x * x * x);
    float e = __expf(y2);
    return x - x * __builtin_amdgcn_rcpf(1.0f + e);
}

struct Frag4x4 { bf16x8 a[4]; bf16x4 bl[4], bh[4]; };
#define F4_STEP(C, N, AADDR, SBO)                                                          \
    do {                                                                                   \
        const unsigned a_ = (AADDR), sb_ = (SBO);                                          \
        _Pragma("unroll") for (int ct = 0; ct < 4; ++ct) {                                 \
            LDS_TR(N.bl[ct], blo3[ct] + sb_, 0);  LDS_TR(N.bh[ct], blo3[ct] + sb_, 1024);  \
            if (ct == 0) LDS_B128(N.a[0], a_, 0);                                          \
            if (ct == 1) LDS_B128(N.a[1], a_, 2048);                                       \
            if (ct == 2) LDS_B128(N.a[2], a_, 4096);                                       \
            if (ct == 3) LDS_B128(N.a[3], a_, 6144);                                       \
            SB0();                                                                         \
            bf16x8 b_ = cat8v(C.bl[ct], C.bh[ct]);                                         \
            _Pragma("unroll") for (int rt = 0; rt < 4; ++rt)                               \
                acc[rt][ct] = MFMA(b_, C.a[rt], acc[rt][ct]);                              \
            SB0();                                                                         \
        }                                                                                  \
    } while (0)
#define F4_READ(N, AADDR, SBO)                                                             \
    do {                                                                                   \
        const unsigned a_ = (AADDR), sb_ = (SBO);                                          \
        _Pragma("unroll") for (int ct = 0; ct < 4; ++ct) {                                 \
            LDS_TR(N.bl[ct], blo3[ct] + sb_, 0);  LDS_TR(N.bh[ct], blo3[ct] + sb_, 1024);  \
        }                                                                                  \
        LDS_B128(N.a[0], a_, 0); LDS_B128(N.a[1], a_, 2048); LDS_B128(N.a[2], a_, 4096); LDS_B128(N.a[3], a_, 6144); \
    } while (0)
#define F4_MMA(C)                                                                          \
    do {                                                                                   \
        _Pragma("unroll") for (int ct = 0; ct < 4; ++ct) {                                 \
            bf16x8 b_ = cat8v(C.bl[ct], C.bh[ct]);                                         \
            _Pragma("unroll") for (int rt = 0; rt < 4; ++rt)                               \
                acc[rt][ct] = MFMA(b_, C.a[rt], acc[rt][ct]);                              \
        }                                                                                  \
    } while (0)
#define F4_WAIT(F)                                                                                            \
    asm volatile("s_waitcnt lgkmcnt(0)"                                                                       \
                 : "+v"(F.a[0]), "+v"(F.a[1]), "+v"(F.a[2]), "+v"(F.a[3]), "+v"(F.bl[0]), "+v"(F.bl[1]),       \
                   "+v"(F.bl[2]), "+v"(F.bl[3]), "+v"(F.bh[0]), "+v"(F.bh[1]), "+v"(F.bh[2]), "+v"(F.bh[3]))

__global__ __launch_bounds__(768, 3) void k3_gate_up(const u16* __restrict__ x, const float2* __restrict__ pstats,
                                                     const u16* __restrict__ wg,
                                                     const float* __restrict__ csum, u16* __restrict__ hbuf) {
    extern __shared__ __attribute__((aligned(16))) char smem[];
    const int tid = threadIdx.x, lane = tid & 63, wave = __builtin_amdgcn_readfirstlane(tid >> 6);
    const int g = lane >> 4, l15 = lane & 15;
    const int rb = blockIdx.x & 7, cb = blockIdx.x >> 3;
    const int row0 = rb * 256, col0 = cb * 64;
    float2* stats = (float2*)(smem + K3_ST);
    STAMP(2, 0);
    if (wave >= 8) {
        const int lw = wave - 8;
        const char* srcA = (const char*)x + (size_t)(rb * 4 + lw) * 10 * 8192 + lane * 16;
        const char* srcB = (const char*)wg + (size_t)cb * 10 * 16384 + lw * 4096 + lane * 16;
        auto stage = [&](int s) {
            char* base = smem + (s % 3) * K3_STG;
#pragma unroll
            for (int i = 0; i < 4; ++i) glds16(srcB + (size_t)s * 16384 + i * 1024, base + 32768 + lw * 4096 + i * 1024);
#pragma unroll
            for (int i = 0; i < 8; ++i) glds16(srcA + (size_t)s * 8192 + i * 1024, base + lw * 8192 + i * 1024);
        };
        stage(0); stage(1);
#pragma unroll 1
        for (int b = 0; b < 10; ++b) {
            wait_stages<12, 1>(9 - b);
            BAR_ONLY();
            if (b + 2 < 10) stage(b + 2);
        }
        return;
    }
    if (tid < 256) stats[tid] = combine_stats(pstats + (size_t)(row0 + tid) * 8);
    asm volatile("s_waitcnt lgkmcnt(0)" ::: "memory");
    const int wr = wave >> 1, wc = wave & 1;
    f32x4 acc[4][4];
#pragma unroll
    for (int i = 0; i < 4; ++i)
#pragma unroll
        for (int j = 0; j < 4; ++j) acc[i][j] = (f32x4){0.f, 0.f, 0.f, 0.f};
    const unsigned sm3 = lds_addr(smem);
    unsigned blo3[4];
    {
        int q = l15 >> 2, p4 = lane & 3;
        int sx = q | ((g & 1) << 2);
#pragma unroll
        for (int ct = 0; ct < 4; ++ct) {
            int nt = (ct >> 1) * 4 + wc * 2 + (ct & 1);
            blo3[ct] = sm3 + 32768 + (8 * g + q) * 256 + ((nt ^ sx) * 32) + p4 * 8;
        }
    }
    unsigned aad3[2];
    {
        int r = wr * 64 + l15;
        aad3[0] = sm3 + r * 128 + (((unsigned)(g ^ (r & 7))) << 4);
        aad3[1] = sm3 + r * 128 + (((unsigned)((4 + g) ^ (r & 7))) << 4);
    }
    Frag4x4 fa, fb;
    BAR_ONLY();
    STAMP(2, 1);
    F4_READ(fa, aad3[0], 0);
    F4_WAIT(fa);
#pragma unroll 1
    for (int s = 0; s < 9; ++s) {
        const unsigned so = (s % 3) * K3_STG;
        F4_STEP(fa, fb, aad3[1] + so, so + 8192);
        F4_WAIT(fb);
        BAR_ONLY();
        const unsigned sn = ((s + 1) % 3) * K3_STG;
        F4_STEP(fb, fa, aad3[0] + sn, sn);
        F4_WAIT(fa);
    }
    F4_STEP(fa, fb, aad3[1], 8192);
    F4_WAIT(fb);
    F4_MMA(fb);
    STAMP(2, 2);
    {
        f32x4 cs[4];
#pragma unroll
        for (int c2 = 0; c2 < 2; ++c2) {
            int col = col0 + (wc * 2 + c2) * 16 + 4 * g;
            cs[c2] = *(const f32x4*)(csum + col);
            cs[2 + c2] = *(const f32x4*)(csum + DF_ + col);
        }
#pragma unroll
        for (int rt = 0; rt < 4; ++rt) {
            int rl = wr * 64 + rt * 16 + l15;
            float2 st = stats[rl];
#pragma unroll
            for (int c2 = 0; c2 < 2; ++c2) {
                int col = col0 + (wc * 2 + c2) * 16 + 4 * g;
                float hv[4];
#pragma unroll
                for (int j = 0; j < 4; ++j) {
                    float gg = rbf((acc[rt][c2][j] - st.x * cs[c2][j]) * st.y);
                    float uu = rbf((acc[rt][2 + c2][j] - st.x * cs[2 + c2][j]) * st.y);
                    hv[j] = rbf(gelu_tanh(gg)) * uu;
                }
                u32x2 v;
                v[0] = pack2(hv[0], hv[1]);
                v[1] = pack2(hv[2], hv[3]);
                {
                    int row = row0 + rl, r = row & 63, cc = col & 63;
                    *(u32x2*)(hbuf + (size_t)((row >> 6) * 27 + cb) * 4096 + r * 64 + (((cc >> 3) ^ (r & 7)) << 3) + (cc & 7)) = v;
                }
            }
        }
    }
    STAMP(2, 3);
}

constexpr int K4_STG = 36864;
constexpr int K4_SMEM = 4 * K4_STG;

__global__ __launch_bounds__(768, 3) void k4_down(const u16* __restrict__ hbuf, const u16* __restrict__ wd,
                                                  const u16* __restrict__ xin, u16* __restrict__ xout,
                                                  u16* __restrict__ xtile, float2* __restrict__ stats) {
    extern __shared__ __attribute__((aligned(16))) char smem[];
    const int tid = threadIdx.x, lane = tid & 63, wave = __builtin_amdgcn_readfirstlane(tid >> 6);
    const int g = lane >> 4, l15 = lane & 15;
    const int rb = (blockIdx.x & 7) * 4 + ((blockIdx.x >> 3) & 3), cb = blockIdx.x >> 5;
    const int row0 = rb * 64, col0 = cb * 80;
    constexpr int NS = 14;
    STAMP(3, 0);

    if (wave >= 8) {
        const int lw = wave - 8;
        const char* srcA = (const char*)hbuf + (size_t)rb * 27 * 8192 + lw * 4096 + lane * 16;
        const char* srcB = (const char*)wd + (size_t)cb * 14 * 20480 + lw * 1024 + lane * 16;
        auto stage = [&](int s) {
            char* base = smem + (s & 3) * K4_STG;
            const int sa = (s == NS - 1 && lw >= 2) ? 2 * s - 1 : 2 * s;
#pragma unroll
            for (int i = 0; i < 4; ++i) glds16(srcA + (size_t)sa * 8192 + i * 1024, base + lw * 4096 + i * 1024);
#pragma unroll
            for (int i = 0; i < 5; ++i) glds16(srcB + (size_t)s * 20480 + i * 4096, base + 16384 + lw * 1024 + i * 4096);
        };
        stage(0); stage(1); stage(2);
#pragma unroll 1
        for (int b = 0; b < NS; ++b) {
            wait_stages<9, 2>(NS - 1 - b);
            BAR_ONLY();
            if (b + 3 < NS) stage(b + 3);
        }
        return;
    }

    u32x2 xres[5];
    if (wave < 4) {
#pragma unroll
        for (int ct = 0; ct < 5; ++ct) xres[ct] = *(const u32x2*)(xin + (size_t)(row0 + wave * 16 + l15) * D_ + col0 + ct * 16 + 4 * g);
    }
    const int wr = wave & 1, kq = wave >> 1;
    f32x4 acc[2][5];
#pragma unroll
    for (int i = 0; i < 2; ++i)
#pragma unroll
        for (int j = 0; j < 5; ++j) acc[i][j] = (f32x4){0.f, 0.f, 0.f, 0.f};
    {
        const unsigned sm = lds_addr(smem);
        unsigned blo, bhi;
        wfrag_addr<160>(sm + 16384 + kq * 5120, lane, blo, bhi);
        const int r = wr * 32 + l15;
        const unsigned aad = sm + (kq >> 1) * 8192 + r * 128 + (((unsigned)(((kq & 1) * 4 + g) ^ (r & 7))) << 4);
        Frag2x5 fa, fb;
        BAR_ONLY();
        G5_READ(fa, aad, 2048, blo, bhi);
        G5_WAIT(fa);
#pragma unroll 1
        for (int s = 0; s < NS - 2; s += 2) {
            BAR_ONLY();
            { const unsigned so = ((s + 1) & 3) * K4_STG; G5_STEP(fa, fb, aad + so, 2048, blo + so, bhi + so); }
            G5_WAIT(fb);
            BAR_ONLY();
            { const unsigned so = ((s + 2) & 3) * K4_STG; G5_STEP(fb, fa, aad + so, 2048, blo + so, bhi + so); }
            G5_WAIT(fa);
        }
        BAR_ONLY();
        { const unsigned so = ((NS - 1) & 3) * K4_STG; G5_STEP(fa, fb, aad + so, 2048, blo + so, bhi + so); }
        G5_WAIT(fb);
        if (kq < 2) G5_MMA(fb);
    }
    __syncthreads();
    float* red = (float*)smem;
#pragma unroll
    for (int rt = 0; rt < 2; ++rt)
#pragma unroll
        for (int ct = 0; ct < 5; ++ct) *(f32x4*)(red + (((kq * 4 + wr * 2 + rt) * 5 + ct) * 64 + lane) * 4) = acc[rt][ct];
    __syncthreads();
    STAMP(3, 1);
    if (wave < 4) {
        const int row = row0 + wave * 16 + l15;
        float vals[20];
#pragma unroll
        for (int ct = 0; ct < 5; ++ct) {
            f32x4 o = *(const f32x4*)(red + ((wave * 5 + ct) * 64 + lane) * 4);
#pragma unroll
            for (int q = 1; q < 4; ++q) o += *(const f32x4*)(red + (((q * 4 + wave) * 5 + ct) * 64 + lane) * 4);
            int col = col0 + ct * 16 + 4 * g;
            vals[ct * 4 + 0] = rbf(lo16(xres[ct][0]) + rbf(o[0]));
            vals[ct * 4 + 1] = rbf(hi16(xres[ct][0]) + rbf(o[1]));
            vals[ct * 4 + 2] = rbf(lo16(xres[ct][1]) + rbf(o[2]));
            vals[ct * 4 + 3] = rbf(hi16(xres[ct][1]) + rbf(o[3]));
            u32x2 v;
            v[0] = pack2(vals[ct * 4 + 0], vals[ct * 4 + 1]);
            v[1] = pack2(vals[ct * 4 + 2], vals[ct * 4 + 3]);
            *(u32x2*)(xout + (size_t)row * D_ + col) = v;
            if (xtile) {
                int r = row & 63, cc = col & 63;
                *(u32x2*)(xtile + (size_t)(rb * 10 + (col >> 6)) * 4096 + r * 64 + (((cc >> 3) ^ (r & 7)) << 3) + (cc & 7)) = v;
            }
        }
        if (xtile) slice_stats(vals, stats + (size_t)row * 8 + cb, g);
    }
    STAMP(3, 2);
}

extern "C" void kernel_launch(void* const* d_in, const int* in_sizes, int n_in,
                              void* d_out, int out_size, void* d_ws, size_t ws_size,
                              hipStream_t stream) {
    const u16* A_x = (const u16*)d_in[0];
    const u16* wqkv = (const u16*)d_in[1];
    const u16* wphi = (const u16*)d_in[2];
    const u16* wo = (const u16*)d_in[3];
    const u16* wg = (const u16*)d_in[5];
    const u16* wu = (const u16*)d_in[6];
    const u16* wd = (const u16*)d_in[7];
    u16* out = (u16*)d_out;

    char* ws = (char*)d_ws;
    u16* x1 = (u16*)(ws);
    u16* x2 = (u16*)(ws + (4u << 20));
    char* phiq = ws + (8u << 20);
    float* kvp = (float*)(ws + (16u << 20));
    u16* kvt = (u16*)(ws + (24u << 20));
    float2* stats = (float2*)(ws + (25u << 20));
    float* csum = (float*)(ws + (26u << 20));
    float2* stats0 = (float2*)(ws + (27u << 20));
    u16* hbuf = (u16*)(ws + (28u << 20));
    u16* x1t = (u16*)(ws + (36u << 20));
    u16* x0t = (u16*)(ws + (52u << 20));
    char* wt = ws + (40u << 20);
    const u16* wq_t = (const u16*)wt;
    const u16* wo_t = (const u16*)(wt + (size_t)WQ_CH * 16);
    const u16* wgu_t = (const u16*)(wt + (size_t)(WQ_CH + WO_CH) * 16);
    const u16* wd_t = (const u16*)(wt + (size_t)(WQ_CH + WO_CH + WGU_CH) * 16);

    (void)hipFuncSetAttribute((const void*)k1_qkv, hipFuncAttributeMaxDynamicSharedMemorySize, K1_SMEM);
    (void)hipFuncSetAttribute((const void*)k2_attn_out, hipFuncAttributeMaxDynamicSharedMemorySize, K2_SMEM);
    (void)hipFuncSetAttribute((const void*)k3_gate_up, hipFuncAttributeMaxDynamicSharedMemorySize, K3_SMEM);
    (void)hipFuncSetAttribute((const void*)k4_down, hipFuncAttributeMaxDynamicSharedMemorySize, K4_SMEM);

    k0_prep<<<K0_XBLK + K0_CSBLK + K0_CPBLK, 256, 0, stream>>>(A_x, wqkv, wo, wg, wu, wd, (u32x4*)wt, csum, x0t, stats0);
    const u16* xin = A_x;
    for (int layer = 0; layer < 2; ++layer) {
        u16* xmid = x1;
        u16* xo = (layer == 0) ? x2 : out;
        k1_qkv<<<256, 768, K1_SMEM, stream>>>(x0t, stats0, wq_t, csum, wphi, phiq, kvp);
        kr_reduce<<<1280, 256, 0, stream>>>(kvp, kvt);
        k2_attn_out<<<256, 768, K2_SMEM, stream>>>(xin, phiq, kvt, wo_t, xmid, x1t, stats);
        k3_gate_up<<<216, 768, K3_SMEM, stream>>>(x1t, stats, wgu_t, csum, hbuf);
        k4_down<<<256, 768, K4_SMEM, stream>>>(hbuf, wd_t, xmid, xo, layer == 0 ? x0t : (u16*)nullptr, stats0);
        xin = x2;
    }
}
```

```cpp
#include <hip/hip_runtime.h>
#include <hip/hip_bf16.h>
#include <stdint.h>

#define STAMP(k, i)
constexpr int S_ = 2048, D_ = 640, H_ = 32, DH_ = 20, DF_ = 1728;
constexpr int N3_ = 3 * D_;

typedef __attribute__((ext_vector_type(8))) short bf16x8;
typedef __attribute__((ext_vector_type(4))) short bf16x4;
typedef __attribute__((ext_vector_type(4))) float f32x4;
typedef __attribute__((ext_vector_type(4))) unsigned int u32x4;
typedef __attribute__((ext_vector_type(2))) unsigned int u32x2;
typedef unsigned short u16;
typedef __attribute__((address_space(3))) bf16x4* lds_b4_ptr;
typedef __attribute__((address_space(3))) void* lds_vptr;
typedef const __attribute__((address_space(1))) void* glb_vptr;

__device__ __forceinline__ float bf2f(u16 v) { return __uint_as_float(((unsigned)v) << 16); }
__device__ __forceinline__ u16 f2bf(float f) { return __builtin_bit_cast(u16, (__bf16)f); }
__device__ __forceinline__ float rbf(float f) { return bf2f(f2bf(f)); }
__device__ __forceinline__ unsigned pack2(float a, float b) { return (unsigned)f2bf(a) | ((unsigned)f2bf(b) << 16); }
__device__ __forceinline__ float lo16(unsigned u) { return __uint_as_float(u << 16); }
__device__ __forceinline__ float hi16(unsigned u) { return __uint_as_float(u & 0xffff0000u); }
__device__ __forceinline__ bf16x4 tr_read(const char* p) { return __builtin_amdgcn_ds_read_tr16_b64_v4i16((lds_b4_ptr)p); }
__device__ __forceinline__ bf16x8 cat8v(bf16x4 a, bf16x4 b) { return __builtin_shufflevector(a, b, 0, 1, 2, 3, 4, 5, 6, 7); }
#define MFMA(a, b, c) __builtin_amdgcn_mfma_f32_16x16x32_bf16((a), (b), (c), 0, 0, 0)

template <int CTRL> __device__ __forceinline__ float dpp_mov(float x) {
    return __int_as_float(__builtin_amdgcn_update_dpp(0, __float_as_int(x), CTRL, 0xf, 0xf, false));
}
__device__ __forceinline__ float sum16(float x) {
    x += dpp_mov<0x128>(x); x += dpp_mov<0x124>(x); x += dpp_mov<0x122>(x); x += dpp_mov<0x121>(x);
    return x;
}
__device__ __forceinline__ void swap16(float x, float& a, float& b) {
    auto r = __builtin_amdgcn_permlane16_swap(__float_as_uint(x), __float_as_uint(x), false, false);
    a = __uint_as_float(r[0]); b = __uint_as_float(r[1]);
}
__device__ __forceinline__ void swap32(float x, float& a, float& b) {
    auto r = __builtin_amdgcn_permlane32_swap(__float_as_uint(x), __float_as_uint(x), false, false);
    a = __uint_as_float(r[0]); b = __uint_as_float(r[1]);
}
__device__ __forceinline__ float sum_g(float x) { float a, b; swap16(x, a, b); x = a + b; swap32(x, a, b); return a + b; }
__device__ __forceinline__ float max_g(float x) { float a, b; swap16(x, a, b); x = fmaxf(a, b); swap32(x, a, b); return fmaxf(a, b); }
__device__ __forceinline__ float min_g(float x) { float a, b; swap16(x, a, b); x = fminf(a, b); swap32(x, a, b); return fminf(a, b); }

__device__ __forceinline__ u32x4 ln8(u32x4 v, float mean_bf, float rinv) {
    u32x4 o;
#pragma unroll
    for (int i = 0; i < 4; ++i) {
        float a = rbf(lo16(v[i]) - mean_bf) * rinv;
        float b = rbf(hi16(v[i]) - mean_bf) * rinv;
        o[i] = pack2(a, b);
    }
    return o;
}
__device__ __forceinline__ void row_stats16(const u32x4 (&c)[5], float& mean_bf, float& rinv) {
    float s = 0.f;
#pragma unroll
    for (int i = 0; i < 5; ++i)
#pragma unroll
        for (int j = 0; j < 4; ++j) { s += lo16(c[i][j]); s += hi16(c[i][j]); }
    s = sum16(s);
    float mean = s * (1.0f / 640.0f);
    float q = 0.f;
#pragma unroll
    for (int i = 0; i < 5; ++i)
#pragma unroll
        for (int j = 0; j < 4; ++j) {
            float a = lo16(c[i][j]) - mean, b = hi16(c[i][j]) - mean;
            q += a * a; q += b * b;
        }
    q = sum16(q);
    float var_bf = rbf(q * (1.0f / 640.0f));
    float den = rbf(sqrtf(rbf(var_bf + 1e-5f)));
    mean_bf = rbf(mean);
    rinv = 1.0f / den;
}

#define RAW_BAR() do { asm volatile("s_waitcnt lgkmcnt(0)" ::: "memory"); __builtin_amdgcn_s_barrier(); asm volatile("" ::: "memory"); } while (0)
__device__ __forceinline__ void glds16(const void* src, char* lds_dst) {
    __builtin_amdgcn_global_load_lds((glb_vptr)src, (lds_vptr)lds_dst, 16, 0, 0);
}
template <int CPR>
__device__ __forceinline__ void wsrc(int ins, int lane, int& k, int& nch) {
    int c = ins * 64 + lane;
    int pos = c / CPR;
    nch = c - pos * CPR;
    k = pos ^ (((pos >> 3) & 1) << 2);
}
#define LDS_TR(dst, addr, off) asm volatile("ds_read_b64_tr_b16 %0, %1 offset:%2" : "=v"(dst) : "v"(addr), "i"(off))
#define LDS_B128(dst, addr, off) asm volatile("ds_read_b128 %0, %1 offset:%2" : "=v"(dst) : "v"(addr), "i"(off))
__device__ __forceinline__ unsigned lds_addr(const void* p) { return (unsigned)(size_t)p; }
template <int RS>
__device__ __forceinline__ void wfrag_addr(unsigned img, int lane, unsigned& lo, unsigned& hi) {
    int g = lane >> 4, q = (lane & 15) >> 2, p = lane & 3;
    int sw = (g & 1) * 4;
    lo = img + (8 * g + sw + q) * RS + p * 8;
    hi = img + (8 * g + (4 ^ sw) + q) * RS + p * 8;
}
template <int N> __device__ __forceinline__ void wait_vm() { asm volatile("s_waitcnt vmcnt(%0)" ::"n"(N) : "memory"); }
template <int PER, int MAXS>
__device__ __forceinline__ void wait_stages(int stages) {
    if (stages >= MAXS) { wait_vm<PER * MAXS>(); return; }
    if constexpr (MAXS > 0) wait_stages<PER, MAXS - 1>(stages);
}

#define BAR_ONLY() do { asm volatile("" ::: "memory"); __builtin_amdgcn_s_barrier(); asm volatile("" ::: "memory"); } while (0)
#define SB0() __builtin_amdgcn_sched_barrier(0)

constexpr int PHT_ = 1280;
constexpr int KVT_H = 21 * 40;

constexpr int WQ_CH = 8 * 10 * 1920;
constexpr int WO_CH = 8 * 5 * 1280;
constexpr int WGU_CH = 27 * 10 * 1024;
constexpr int WD_CH = 8 * 14 * 1280;
constexpr int K0_CH = WQ_CH + WO_CH + WGU_CH + WD_CH;
constexpr int K0_CPBLK = (K0_CH + 1023) / 1024;
constexpr int K0_CSBLK = 54 + 30;
constexpr int K0_XBLK = 128;

__device__ __forceinline__ const u16* k0_src(int cc, const u16* wqkv, const u16* wo, const u16* wg, const u16* wu,
                                             const u16* wd, bool& zero) {
    zero = false;
    if (cc < WQ_CH) {
        int blk = cc / 1920, r = cc - blk * 1920;
        int hg = blk / 10, st = blk - hg * 10;
        int pos = r / 30, nch = r - pos * 30;
        int k = pos ^ (((pos >> 3) & 1) << 2);
        int sec = nch / 10, w = nch - sec * 10;
        return wqkv + (size_t)(st * 64 + k) * N3_ + sec * D_ + hg * 80 + w * 8;
    }
    cc -= WQ_CH;
    if (cc < WO_CH) {
        int blk = cc / 1280, r = cc - blk * 1280;
        int cb = blk / 5, st = blk - cb * 5;
        int pos = r / 10, nch = r - pos * 10;
        int k = pos ^ (((pos >> 3) & 1) << 2);
        return wo + (size_t)(st * 128 + k) * D_ + cb * 80 + nch * 8;
    }
    cc -= WO_CH;
    if (cc < WGU_CH) {
        int blk = cc >> 10, r = cc & 1023;
        int cb = blk / 10, st = blk - cb * 10;
        int k = r >> 4, cpos = r & 15;
        int sx = (k & 3) | (((k >> 3) & 1) << 2);
        int nch = (((cpos >> 1) ^ sx) << 1) | (cpos & 1);
        const u16* w = (nch < 8) ? wg : wu;
        return w + (size_t)(st * 64 + k) * DF_ + cb * 64 + (nch & 7) * 8;
    }
    cc -= WGU_CH;
    int blk = cc / 1280, r = cc - blk * 1280;
    int cb = blk / 14, st = blk - cb * 14;
    int pos = r / 10, nch = r - pos * 10;
    int k = st * 128 + (pos ^ (((pos >> 3) & 1) << 2));
    zero = (k >= DF_);
    if (zero) k = 0;
    return wd + (size_t)k * D_ + cb * 80 + nch * 8;
}

__global__ __launch_bounds__(256) void k0_prep(const u16* __restrict__ x, const u16* __restrict__ wqkv,
                                               const u16* __restrict__ wo, const u16* __restrict__ wg,
                                               const u16* __restrict__ wu, const u16* __restrict__ wd,
                                               u32x4* __restrict__ wt, float* __restrict__ csum,
                                               u16* __restrict__ xtile, float2* __restrict__ stats) {
    __shared__ float red[2048];
    const int tid = threadIdx.x, bid = blockIdx.x;
    if (bid < K0_XBLK) {
        const int lane = tid & 63, g = lane >> 4, l15 = lane & 15;
        const int row = bid * 16 + (tid >> 6) * 4 + g;
        u32x4 c[5];
#pragma unroll
        for (int i = 0; i < 5; ++i) c[i] = *(const u32x4*)(x + (size_t)row * D_ + (l15 + 16 * i) * 8);
        float sm = 0.f;
#pragma unroll
        for (int i = 0; i < 5; ++i)
#pragma unroll
            for (int j = 0; j < 4; ++j) { sm += lo16(c[i][j]); sm += hi16(c[i][j]); }
        sm = sum16(sm);
        float mean = sm * (1.0f / 640.0f), q = 0.f;
#pragma unroll
        for (int i = 0; i < 5; ++i)
#pragma unroll
            for (int j = 0; j < 4; ++j) {
                float a = lo16(c[i][j]) - mean, b = hi16(c[i][j]) - mean;
                q += a * a; q += b * b;
            }
        q = sum16(q);
        if (l15 < 8) stats[(size_t)row * 8 + l15] = make_float2(mean, q * 0.125f);
        const int r = row & 63;
#pragma unroll
        for (int i = 0; i < 5; ++i) {
            int ch = l15 + 16 * i;
            *(u32x4*)(xtile + (size_t)((row >> 6) * 10 + (ch >> 3)) * 4096 + r * 64 + (((ch & 7) ^ (r & 7)) << 3)) = c[i];
        }
    } else if (bid < K0_XBLK + K0_CSBLK) {
        const int cbk = bid - K0_XBLK;
        const u16* w; int ld, col0, obase;
        if (cbk < 54) { w = (cbk < 27) ? wg : wu; ld = DF_; col0 = (cbk % 27) * 64; obase = (cbk < 27 ? 0 : DF_) + col0; }
        else { w = wqkv; ld = N3_; col0 = (cbk - 54) * 64; obase = 2 * DF_ + col0; }
        const int c8 = tid & 7, rg = tid >> 3;
        float s[8] = {0.f, 0.f, 0.f, 0.f, 0.f, 0.f, 0.f, 0.f};
        u32x4 v[20];
#pragma unroll
        for (int i = 0; i < 20; ++i) v[i] = *(const u32x4*)(w + (size_t)(rg * 20 + i) * ld + col0 + c8 * 8);
#pragma unroll
        for (int i = 0; i < 20; ++i)
#pragma unroll
            for (int j = 0; j < 4; ++j) { s[2 * j] += lo16(v[i][j]); s[2 * j + 1] += hi16(v[i][j]); }
#pragma unroll
        for (int j = 0; j < 8; ++j) red[rg * 64 + c8 * 8 + j] = s[j];
        __syncthreads();
        if (tid < 64) {
            float t = 0.f;
#pragma unroll
            for (int i = 0; i < 32; ++i) t += red[i * 64 + tid];
            csum[obase + tid] = t;
        }
    } else {
        const int c0 = (bid - K0_XBLK - K0_CSBLK) * 1024 + tid;
        u32x4 v[4];
        bool z[4];
#pragma unroll
        for (int i = 0; i < 4; ++i) {
            int c = c0 + 256 * i; if (c >= K0_CH) c = K0_CH - 1;
            v[i] = *(const u32x4*)k0_src(c, wqkv, wo, wg, wu, wd, z[i]);
        }
#pragma unroll
        for (int i = 0; i < 4; ++i) {
            int c = c0 + 256 * i;
            if (c < K0_CH) wt[c] = z[i] ? (u32x4){0u, 0u, 0u, 0u} : v[i];
        }
    }
}

__device__ __forceinline__ float2 combine_stats(const float2* ps8) {
    const f32x4* ps = (const f32x4*)ps8;
    f32x4 p0 = ps[0], p1 = ps[1], p2 = ps[2], p3 = ps[3];
    float mean = (p0[0] + p0[2] + p1[0] + p1[2] + p2[0] + p2[2] + p3[0] + p3[2]) * 0.125f;
    float m2 = p0[1] + p0[3] + p1[1] + p1[3] + p2[1] + p2[3] + p3[1] + p3[3];
    float dm = 0.f, t;
    t = p0[0] - mean; dm += t * t; t = p0[2] - mean; dm += t * t;
    t = p1[0] - mean; dm += t * t; t = p1[2] - mean; dm += t * t;
    t = p2[0] - mean; dm += t * t; t = p2[2] - mean; dm += t * t;
    t = p3[0] - mean; dm += t * t; t = p3[2] - mean; dm += t * t;
    float var_bf = rbf((m2 + 80.0f * dm) * (1.0f / 640.0f));
    float den = rbf(sqrtf(rbf(var_bf + 1e-5f)));
    return make_float2(rbf(mean), 1.0f / den);
}

constexpr int K1_STG = 38912;
constexpr int K1_WP = 65536;
constexpr int K1_ST = 4 * K1_STG;
constexpr int K1_SMEM = K1_ST + 512;
constexpr int K1_T = 0;
constexpr int K1_TRS = 144;
constexpr int K1_PK = 256 * K1_TRS;
constexpr int K1_PKRS = 336;

struct Frag2x4 { bf16x8 a[2]; bf16x4 bl[4], bh[4]; };
#define K1_READ_CT(N, ct, so) do { LDS_TR(N.bl[ct], blo + (so), ct * 32); LDS_TR(N.bh[ct], bhi + (so), ct * 32); } while (0)
#define K1_WAIT(F)                                                                                      \
    asm volatile("s_waitcnt lgkmcnt(0)"                                                                 \
                 : "+v"(F.a[0]), "+v"(F.a[1]), "+v"(F.bl[0]), "+v"(F.bl[1]), "+v"(F.bl[2]), "+v"(F.bl[3]), \
                   "+v"(F.bh[0]), "+v"(F.bh[1]), "+v"(F.bh[2]), "+v"(F.bh[3]))
#define K1_READ(N, AA, SO)                                                                              \
    do {                                                                                                \
        const unsigned aa_ = (AA), so_ = (SO);                                                          \
        LDS_B128(N.a[0], aa_, 0); LDS_B128(N.a[1], aa_, 2048);                                          \
        K1_READ_CT(N, 0, so_); K1_READ_CT(N, 1, so_); K1_READ_CT(N, 2, so_); K1_READ_CT(N, 3, so_);     \
    } while (0)
#define K1_MMA_CT(C, ct)                                                                                \
    do {                                                                                                \
        bf16x8 b_ = cat8v(C.bl[ct], C.bh[ct]);                                                          \
        acc[0][ct] = MFMA(C.a[0], b_, acc[0][ct]);                                                      \
        acc[1][ct] = MFMA(C.a[1], b_, acc[1][ct]);                                                      \
    } while (0)
#define K1_STEP(C, N, AA, SO)                                                                           \
    do {                                                                                                \
        const unsigned aa_ = (AA), so_ = (SO);                                                          \
        LDS_B128(N.a[0], aa_, 0); K1_READ_CT(N, 0, so_); SB0(); K1_MMA_CT(C, 0); SB0();                 \
        LDS_B128(N.a[1], aa_, 2048); K1_READ_CT(N, 1, so_); SB0(); K1_MMA_CT(C, 1); SB0();              \
        K1_READ_CT(N, 2, so_); SB0(); K1_MMA_CT(C, 2); SB0();                                           \
        K1_READ_CT(N, 3, so_); SB0(); K1_MMA_CT(C, 3); SB0();                                           \
    } while (0)

__global__ __launch_bounds__(768, 3) void k1_qkv(const u16* __restrict__ xt, const float2* __restrict__ pstats,
                                                 const u16* __restrict__ wqkv, const float* __restrict__ csum,
                                                 const u16* __restrict__ wphi, char* __restrict__ phiq,
                                                 float* __restrict__ kvp) {
    extern __shared__ __attribute__((aligned(16))) char smem[];
    const int tid = threadIdx.x, lane = tid & 63, wave = __builtin_amdgcn_readfirstlane(tid >> 6);
    const int g = lane >> 4, l15 = lane & 15;
    const int hg = blockIdx.x & 7, rb = blockIdx.x >> 3;
    const int row0 = rb * 64;
    float2* stats = (float2*)(smem + K1_ST);
    STAMP(0, 0);

    if (wave >= 8) {
        const int lw = wave - 8;
        const char* srcA = (const char*)xt + (size_t)rb * 10 * 8192 + lw * 2048 + lane * 16;
        const char* srcB = (const char*)wqkv + (size_t)hg * 10 * 30720 + lw * 1024 + lane * 16;
        auto stage = [&](int s) {
            char* base = smem + (s & 3) * K1_STG;
#pragma unroll
            for (int i = 0; i < 2; ++i) glds16(srcA + (size_t)s * 8192 + i * 1024, base + lw * 2048 + i * 1024);
#pragma unroll
            for (int i = 0; i < 7; ++i) glds16(srcB + (size_t)s * 30720 + i * 4096, base + 8192 + lw * 1024 + i * 4096);
            if (lw < 2) glds16(srcB + (size_t)s * 30720 + 7 * 4096, base + 8192 + lw * 1024 + 7 * 4096);
        };
        stage(0); stage(1); stage(2);
#pragma unroll 1
        for (int b = 0; b < 10; ++b) {
            if (lw < 2) wait_stages<10, 2>(9 - b); else wait_stages<9, 2>(9 - b);
            BAR_ONLY();
            if (b + 3 < 10) stage(b + 3);
        }
        return;
    }
    __builtin_amdgcn_s_setprio(2);

    if (tid < 64) stats[tid] = combine_stats(pstats + (size_t)(row0 + tid) * 8);
    bf16x8 wpfrag;
    {
        int hh = tid >> 7, t = (tid >> 6) & 1;
        int wf = (t == 0) ? (5 * (l15 >> 2) + (l15 & 3)) : (((l15 & 3) == 0) ? (5 * (l15 >> 2) + 4) : -1);
        int fc = wf < 0 ? 0 : wf;
        u16 wpv[8];
#pragma unroll
        for (int j = 0; j < 8; ++j) {
            int d = 8 * g + j; if (d > 19) d = 19;
            wpv[j] = wphi[((hg * 4 + hh) * DH_ + d) * DH_ + fc];
        }
        bf16x8 v;
#pragma unroll
        for (int j = 0; j < 8; ++j) v[j] = (wf >= 0 && 8 * g + j < DH_) ? (short)wpv[j] : (short)0;
        wpfrag = v;
    }
    asm volatile("s_waitcnt lgkmcnt(0)" ::: "memory");

    const int wr = wave >> 2, wc = wave & 3;
    f32x4 acc[2][4];
#pragma unroll
    for (int i = 0; i < 2; ++i)
#pragma unroll
        for (int j = 0; j < 4; ++j) acc[i][j] = (f32x4){0.f, 0.f, 0.f, 0.f};
    const int nct = (wc == 3) ? 3 : 4;
    {
        const unsigned sm = lds_addr(smem);
        unsigned blo, bhi;
        wfrag_addr<480>(sm + 8192 + wc * 4 * 32, lane, blo, bhi);
        const int r = wr * 32 + l15;
        const unsigned aad0 = sm + r * 128 + (((unsigned)(g ^ (r & 7))) << 4);
        const unsigned aad1 = sm + r * 128 + (((unsigned)((4 + g) ^ (r & 7))) << 4);
        Frag2x4 fa, fb;
        BAR_ONLY();
        STAMP(0, 1);
        K1_READ(fa, aad0, 0);
        K1_WAIT(fa);
#pragma unroll 1
        for (int s = 0; s < 9; ++s) {
            const unsigned so = (s & 3) * K1_STG;
            K1_STEP(fa, fb, aad1 + so, so + 32 * 480);
            K1_WAIT(fb);
            BAR_ONLY();
            const unsigned sn = ((s + 1) & 3) * K1_STG;
            K1_STEP(fb, fa, aad0 + sn, sn);
            K1_WAIT(fa);
        }
        K1_STEP(fa, fb, aad1 + K1_STG, K1_STG + 32 * 480);
        K1_WAIT(fb);
        K1_MMA_CT(fb, 0); K1_MMA_CT(fb, 1); K1_MMA_CT(fb, 2); K1_MMA_CT(fb, 3);
    }
    __syncthreads();
    STAMP(0, 2);

    *(bf16x8*)(smem + K1_WP + tid * 16) = wpfrag;
    {
        float2 st[2][4];
#pragma unroll
        for (int rt = 0; rt < 2; ++rt)
#pragma unroll
            for (int j = 0; j < 4; ++j) st[rt][j] = stats[wr * 32 + rt * 16 + 4 * g + j];
#pragma unroll
        for (int ct = 0; ct < 4; ++ct) {
            if (ct < nct) {
                int col = (wc * 4 + ct) * 16 + l15;
                int sec = col / 80;
                float cs = csum[2 * DF_ + sec * D_ + hg * 80 + (col - sec * 80)];
#pragma unroll
                for (int rt = 0; rt < 2; ++rt) {
                    float y[4];
#pragma unroll
                    for (int j = 0; j < 4; ++j) y[j] = (acc[rt][ct][j] - st[rt][j].x * cs) * st[rt][j].y;
                    u32x2 v;
                    v[0] = pack2(y[0], y[1]);
                    v[1] = pack2(y[2], y[3]);
                    *(u32x2*)(smem + K1_T + col * K1_TRS + (wr * 32 + rt * 16 + 4 * g) * 2) = v;
                }
            }
        }
    }
    {
        int c = 240 + (tid >> 5), w = tid & 31;
        *(unsigned*)(smem + K1_T + c * K1_TRS + w * 4) = (c == 240) ? 0x3f803f80u : 0u;
    }
    __syncthreads();
    STAMP(0, 3);

    {
        const int q4 = l15 >> 2, p4 = lane & 3;
        const int rowt = (wave >> 1) * 16, sec = wave & 1;
        float p[4][5];
#pragma unroll
        for (int hh = 0; hh < 4; ++hh) {
            bf16x8 wp0 = *(const bf16x8*)(smem + K1_WP + ((hh * 2 + 0) * 64 + lane) * 16);
            bf16x8 wp1 = *(const bf16x8*)(smem + K1_WP + ((hh * 2 + 1) * 64 + lane) * 16);
            int c0 = sec * 80 + hh * 20 + 8 * g + q4;
            const char* tb = smem + K1_T + c0 * K1_TRS + (rowt + 4 * p4) * 2;
            bf16x8 b = cat8v(tr_read(tb), tr_read(tb + 4 * K1_TRS));
            f32x4 z = {0.f, 0.f, 0.f, 0.f};
            f32x4 d0 = MFMA(wp0, b, z);
            f32x4 d1 = MFMA(wp1, b, z);
            p[hh][0] = rbf(d0[0]); p[hh][1] = rbf(d0[1]); p[hh][2] = rbf(d0[2]); p[hh][3] = rbf(d0[3]); p[hh][4] = rbf(d1[0]);
        }
#pragma unroll
        for (int hh = 0; hh < 4; ++hh) {
            float mx = fmaxf(fmaxf(fmaxf(p[hh][0], p[hh][1]), fmaxf(p[hh][2], p[hh][3])), p[hh][4]);
            float mn = fminf(fminf(fminf(p[hh][0], p[hh][1]), fminf(p[hh][2], p[hh][3])), p[hh][4]);
            mx = max_g(mx);
            mn = min_g(mn);
            float ep[5], en[5], sp = 0.f, sn = 0.f;
#pragma unroll
            for (int i = 0; i < 5; ++i) {
                ep[i] = __expf(p[hh][i] - mx); sp += ep[i];
                en[i] = __expf(mn - p[hh][i]); sn += en[i];
            }
            sp = sum_g(sp);
            sn = sum_g(sn);
            float ip = __builtin_amdgcn_rcpf(sp), in_ = __builtin_amdgcn_rcpf(sn);
            u32x4 o;
            o[0] = pack2(ep[0] * ip, ep[1] * ip);
            o[1] = pack2(ep[2] * ip, ep[3] * ip);
            o[2] = pack2(ep[4] * ip, en[0] * in_);
            o[3] = pack2(en[1] * in_, en[2] * in_);
            unsigned o4 = pack2(en[3] * in_, en[4] * in_);
            if (sec == 0) {
                char* dst = phiq + (size_t)(((row0 + rowt) >> 4) * H_ + hg * 4 + hh) * PHT_;
                *(u32x4*)(dst + lane * 16) = o;
                *(unsigned*)(dst + 1024 + l15 * 16 + g * 4) = o4;
            } else {
                char* dst = smem + K1_PK + (rowt + l15) * K1_PKRS + hh * 80;
                *(u32x4*)(dst + 16 * g) = o;
                *(unsigned*)(dst + 64 + 4 * g) = o4;
            }
        }
    }
    __syncthreads();
    STAMP(0, 4);

    {
        const int hh = wave >> 1, dt = wave & 1;
        const int q4 = l15 >> 2, p4 = lane & 3;
        f32x4 kacc[3];
#pragma unroll
        for (int ft = 0; ft < 3; ++ft) kacc[ft] = (f32x4){0.f, 0.f, 0.f, 0.f};
#pragma unroll
        for (int ks = 0; ks < 2; ++ks) {
            int c = 160 + hh * 20 + dt * 16 + l15;
            if (dt == 1 && l15 >= 4) c = 240;
            bf16x8 b = *(const bf16x8*)(smem + K1_T + c * K1_TRS + (32 * ks + 8 * g) * 2);
#pragma unroll
            for (int ft = 0; ft < 3; ++ft) {
                const char* pb = smem + K1_PK + (32 * ks + 8 * g + q4) * K1_PKRS + (hh * 40 + 16 * ft + 4 * p4) * 2;
                bf16x8 a = cat8v(tr_read(pb), tr_read(pb + 4 * K1_PKRS));
                kacc[ft] = MFMA(a, b, kacc[ft]);
            }
        }
        float* dst = kvp + (size_t)(rb * H_ + hg * 4 + hh) * 40 * 32;
#pragma unroll
        for (int ft = 0; ft < 3; ++ft)
#pragma unroll
            for (int j = 0; j < 4; ++j) {
                int f = 16 * ft + 4 * g + j;
                if (f < 40) dst[f * 32 + dt * 16 + l15] = kacc[ft][j];
            }
    }
    STAMP(0, 5);
}

__global__ __launch_bounds__(256) void kr_reduce(const float* __restrict__ kvp, u16* __restrict__ kvt) {
    __shared__ float red[256];
    const int tid = threadIdx.x;
    const int bid = blockIdx.x;
    const int d = tid & 31, part = tid >> 5;
    const int h = bid / 40, f = bid - h * 40;
    float s = 0.f;
#pragma unroll
    for (int i = 0; i < 4; ++i) s += kvp[((size_t)((part * 4 + i) * H_ + h) * 40 + f) * 32 + d];
    red[tid] = s;
    __syncthreads();
    if (tid < 21) {
        float t = 0.f;
#pragma unroll
        for (int i = 0; i < 8; ++i) t += red[i * 32 + tid];
        kvt[h * KVT_H + tid * 40 + f] = f2bf(t);
    }
}

struct Frag2x5 { bf16x8 a[2]; bf16x4 bl[5], bh[5]; };
#define G5_READ_CT(N, ct) do { LDS_TR(N.bl[ct], l_, ct * 32); LDS_TR(N.bh[ct], h_, ct * 32); } while (0)
#define G5_MMA_CT(C, ct)                                                                   \
    do {                                                                                   \
        bf16x8 b_ = cat8v(C.bl[ct], C.bh[ct]);                                             \
        acc[0][ct] = MFMA(b_, C.a[0], acc[0][ct]);                                         \
        acc[1][ct] = MFMA(b_, C.a[1], acc[1][ct]);                                         \
    } while (0)
#define G5_READ(N, AADDR, AOFF, BLO, BHI)                                                  \
    do {                                                                                   \
        const unsigned a_ = (AADDR), l_ = (BLO), h_ = (BHI);                               \
        LDS_B128(N.a[0], a_, 0); LDS_B128(N.a[1], a_, AOFF);                               \
        G5_READ_CT(N, 0); G5_READ_CT(N, 1); G5_READ_CT(N, 2); G5_READ_CT(N, 3); G5_READ_CT(N, 4); \
    } while (0)
#define G5_STEP(C, N, AADDR, AOFF, BLO, BHI)                                               \
    do {                                                                                   \
        const unsigned a_ = (AADDR), l_ = (BLO), h_ = (BHI);                               \
        LDS_B128(N.a[0], a_, 0); G5_READ_CT(N, 0); SB0(); G5_MMA_CT(C, 0); SB0();          \
        LDS_B128(N.a[1], a_, AOFF); G5_READ_CT(N, 1); SB0(); G5_MMA_CT(C, 1); SB0();       \
        G5_READ_CT(N, 2); SB0(); G5_MMA_CT(C, 2); SB0();                                   \
        G5_READ_CT(N, 3); SB0(); G5_MMA_CT(C, 3); SB0();                                   \
        G5_READ_CT(N, 4); SB0(); G5_MMA_CT(C, 4); SB0();                                   \
    } while (0)
#define G5_MMA(C) do { G5_MMA_CT(C, 0); G5_MMA_CT(C, 1); G5_MMA_CT(C, 2); G5_MMA_CT(C, 3); G5_MMA_CT(C, 4); } while (0)
#define G5_WAIT(F)                                                                                         \
    asm volatile("s_waitcnt lgkmcnt(0)"                                                                    \
                 : "+v"(F.a[0]), "+v"(F.a[1]), "+v"(F.bl[0]), "+v"(F.bl[1]), "+v"(F.bl[2]), "+v"(F.bl[3]),  \
                   "+v"(F.bl[4]), "+v"(F.bh[0]), "+v"(F.bh[1]), "+v"(F.bh[2]), "+v"(F.bh[3]), "+v"(F.bh[4]))

__device__ __forceinline__ void slice_stats(const float (&v)[20], float2* dst, int g) {
    float s = 0.f;
#pragma unroll
    for (int i = 0; i < 20; ++i) s += v[i];
    s = sum_g(s);
    float m = s * (1.0f / 80.0f);
    float q = 0.f;
#pragma unroll
    for (int i = 0; i < 20; ++i) { float t = v[i] - m; q += t * t; }
    q = sum_g(q);
    if (g == 0) *dst = make_float2(m, q);
}

constexpr int K2_O = 0;
constexpr int K2_B = 40960;
constexpr int K2_STG = 40960;
constexpr int K2_SMEM = K2_B + 3 * K2_STG;

__global__ __launch_bounds__(768, 3) void k2_attn_out(const u16* __restrict__ x, const char* __restrict__ phiq,
                                                      const u16* __restrict__ kvt, const u16* __restrict__ wo,
                                                      u16* __restrict__ xout, u16* __restrict__ xtile,
                                                      float2* __restrict__ stats) {
    extern __shared__ __attribute__((aligned(16))) char smem[];
    const int tid = threadIdx.x, lane = tid & 63, wave = __builtin_amdgcn_readfirstlane(tid >> 6);
    const int g = lane >> 4, l15 = lane & 15;
    const int rb = (blockIdx.x & 7) * 8 + ((blockIdx.x >> 3) & 7), cp = blockIdx.x >> 6;
    const int row0 = rb * 32;
    STAMP(1, 0);

    if (wave >= 8) {
        const int lw = wave - 8;
        const char* src = (const char*)wo + (size_t)(cp * 2 + (lw >> 1)) * 5 * 20480 + (lw & 1) * 10240 + lane * 16;
        auto stage = [&](int s) {
            char* base = smem + K2_B + (s % 3) * K2_STG + lw * 10240;
#pragma unroll
            for (int i = 0; i < 10; ++i) glds16(src + (size_t)s * 20480 + i * 1024, base + i * 1024);
        };
        stage(0); stage(1); stage(2);
#pragma unroll 1
        for (int b = 0; b < 5; ++b) {
            wait_stages<10, 1>(4 - b);
            BAR_ONLY();
            if (b >= 1 && b + 2 < 5) stage(b + 2);
        }
        return;
    }
    __builtin_amdgcn_s_setprio(2);

    u32x2 xres[5];
    if (wave < 4) {
#pragma unroll
        for (int ct = 0; ct < 5; ++ct)
            xres[ct] = *(const u32x2*)(x + (size_t)(row0 + (wave & 1) * 16 + l15) * D_ + (cp * 2 + (wave >> 1)) * 80 + ct * 16 + 4 * g);
    }
    {
        struct HF { bf16x8 a[2][2], b0[2], b1[2]; };
        auto load_head = [&](HF& F, int h) {
#pragma unroll
            for (int rt = 0; rt < 2; ++rt) {
                const char* pr = phiq + (size_t)((rb * 2 + rt) * H_ + h) * PHT_;
                F.b0[rt] = *(const bf16x8*)(pr + lane * 16);
                F.b1[rt] = *(const bf16x8*)(pr + 1024 + l15 * 16);
            }
#pragma unroll
            for (int dt = 0; dt < 2; ++dt) {
                int d = 16 * dt + l15; if (d > 20) d = 20;
                F.a[dt][0] = *(const bf16x8*)(kvt + h * KVT_H + d * 40 + 8 * g);
                F.a[dt][1] = *(const bf16x8*)(kvt + h * KVT_H + d * 40 + 32);
            }
        };
        auto do_head = [&](const HF& F, int h) {
            f32x4 d0[2], d1[2];
            const f32x4 z = {0.f, 0.f, 0.f, 0.f};
#pragma unroll
            for (int rt = 0; rt < 2; ++rt) { d0[rt] = MFMA(F.a[0][0], F.b0[rt], z); d1[rt] = MFMA(F.a[1][0], F.b0[rt], z); }
#pragma unroll
            for (int rt = 0; rt < 2; ++rt) {
                bf16x8 bb1 = F.b1[rt];
                if (g != 0) bb1 = (bf16x8){0, 0, 0, 0, 0, 0, 0, 0};
                d0[rt] = MFMA(F.a[0][1], bb1, d0[rt]);
                d1[rt] = MFMA(F.a[1][1], bb1, d1[rt]);
            }
            float rd[2];
#pragma unroll
            for (int rt = 0; rt < 2; ++rt) rd[rt] = sum_g((g == 1) ? d1[rt][0] : 0.f);
#pragma unroll
            for (int rt = 0; rt < 2; ++rt) rd[rt] = __builtin_amdgcn_rcpf(rbf(rbf(rd[rt]) + 1e-6f));
            const int bo = (h * 20 + 4 * g) * 2, bo2 = (h * 20 + 16) * 2;
#pragma unroll
            for (int rt = 0; rt < 2; ++rt) {
                int r = rt * 16 + l15;
                u32x2 v;
                v[0] = pack2(rbf(d0[rt][0]) * rd[rt], rbf(d0[rt][1]) * rd[rt]);
                v[1] = pack2(rbf(d0[rt][2]) * rd[rt], rbf(d0[rt][3]) * rd[rt]);
                *(u32x2*)(smem + K2_O + r * 1280 + (((bo >> 4) ^ (r & 15)) << 4) + (bo & 15)) = v;
                if (g == 0) {
                    v[0] = pack2(rbf(d1[rt][0]) * rd[rt], rbf(d1[rt][1]) * rd[rt]);
                    v[1] = pack2(rbf(d1[rt][2]) * rd[rt], rbf(d1[rt][3]) * rd[rt]);
                    *(u32x2*)(smem + K2_O + r * 1280 + (((bo2 >> 4) ^ (r & 15)) << 4) + (bo2 & 15)) = v;
                }
            }
        };
        HF f0, f1, f2, f3;
        const int h0 = wave * 4;
        load_head(f0, h0);
        load_head(f1, h0 + 1);
        load_head(f2, h0 + 2);
        load_head(f3, h0 + 3);
        do_head(f0, h0);
        do_head(f1, h0 + 1);
        do_head(f2, h0 + 2);
        do_head(f3, h0 + 3);
    }
    asm volatile("s_waitcnt lgkmcnt(0)" ::: "memory");
    STAMP(1, 1);

    const int ch = wave & 1, kq = wave >> 1;
    f32x4 acc[2][5];
#pragma unroll
    for (int i = 0; i < 2; ++i)
#pragma unroll
        for (int j = 0; j < 5; ++j) acc[i][j] = (f32x4){0.f, 0.f, 0.f, 0.f};
    {
        const unsigned sm = lds_addr(smem);
        unsigned blo, bhi;
        wfrag_addr<160>(sm + K2_B + ch * 20480 + kq * 5120, lane, blo, bhi);
        const unsigned aad = sm + K2_O + l15 * 1280;
#define K2_AA(s) (aad + (((unsigned)(((s) * 16 + kq * 4 + g) ^ l15)) << 4))
        Frag2x5 fa, fb;
        BAR_ONLY();
        G5_READ(fa, K2_AA(0), 16 * 1280, blo, bhi);
        G5_WAIT(fa);
        BAR_ONLY();
        G5_STEP(fa, fb, K2_AA(1), 16 * 1280, blo + K2_STG, bhi + K2_STG);
        G5_WAIT(fb);
        BAR_ONLY();
        G5_STEP(fb, fa, K2_AA(2), 16 * 1280, blo + 2 * K2_STG, bhi + 2 * K2_STG);
        G5_WAIT(fa);
        BAR_ONLY();
        G5_STEP(fa, fb, K2_AA(3), 16 * 1280, blo, bhi);
        G5_WAIT(fb);
        BAR_ONLY();
        G5_STEP(fb, fa, K2_AA(4), 16 * 1280, blo + K2_STG, bhi + K2_STG);
        G5_WAIT(fa);
        G5_MMA(fa);
    }
    __syncthreads();
    float* red = (float*)smem;
#pragma unroll
    for (int rt = 0; rt < 2; ++rt)
#pragma unroll
        for (int ct = 0; ct < 5; ++ct) *(f32x4*)(red + (((kq * 4 + ch * 2 + rt) * 5 + ct) * 64 + lane) * 4) = acc[rt][ct];
    __syncthreads();
    STAMP(1, 2);
    if (wave < 4) {
        const int rt = wave & 1, hc = wave >> 1;
        const int row = row0 + rt * 16 + l15;
        const int cb = cp * 2 + hc, col0 = cb * 80;
        const int slot = hc * 2 + rt;
        float vals[20];
#pragma unroll
        for (int ct = 0; ct < 5; ++ct) {
            f32x4 o = *(const f32x4*)(red + ((slot * 5 + ct) * 64 + lane) * 4);
#pragma unroll
            for (int q = 1; q < 4; ++q) o += *(const f32x4*)(red + (((q * 4 + slot) * 5 + ct) * 64 + lane) * 4);
            int col = col0 + ct * 16 + 4 * g;
            vals[ct * 4 + 0] = rbf(lo16(xres[ct][0]) + rbf(o[0]));
            vals[ct * 4 + 1] = rbf(hi16(xres[ct][0]) + rbf(o[1]));
            vals[ct * 4 + 2] = rbf(lo16(xres[ct][1]) + rbf(o[2]));
            vals[ct * 4 + 3] = rbf(hi16(xres[ct][1]) + rbf(o[3]));
            u32x2 v;
            v[0] = pack2(vals[ct * 4 + 0], vals[ct * 4 + 1]);
            v[1] = pack2(vals[ct * 4 + 2], vals[ct * 4 + 3]);
            *(u32x2*)(xout + (size_t)row * D_ + col) = v;
            int r = row & 63, cc = col & 63;
            *(u32x2*)(xtile + (size_t)((row >> 6) * 10 + (col >> 6)) * 4096 + r * 64 + (((cc >> 3) ^ (r & 7)) << 3) + (cc & 7)) = v;
        }
        slice_stats(vals, stats + (size_t)row * 8 + cb, g);
    }
    STAMP(1, 3);
}

constexpr int K3_STG = 49152;
constexpr int K3_ST = 3 * K3_STG;
constexpr int K3_SMEM = K3_ST + 2048;

__device__ __forceinline__ float gelu_tanh(float x) {
    float y2 = 1.5957691216057308f * (x + 0.044715f * x * x * x);
    float e = __expf(y2);
    return x - x * __builtin_amdgcn_rcpf(1.0f + e);
}

struct Frag4x4 { bf16x8 a[4]; bf16x4 bl[4], bh[4]; };
#define F4_STEP(C, N, AADDR, SBO)                                                          \
    do {                                                                                   \
        const unsigned a_ = (AADDR), sb_ = (SBO);                                          \
        _Pragma("unroll") for (int ct = 0; ct < 4; ++ct) {                                 \
            LDS_TR(N.bl[ct], blo3[ct] + sb_, 0);  LDS_TR(N.bh[ct], blo3[ct] + sb_, 1024);  \
            if (ct == 0) LDS_B128(N.a[0], a_, 0);                                          \
            if (ct == 1) LDS_B128(N.a[1], a_, 2048);                                       \
            if (ct == 2) LDS_B128(N.a[2], a_, 4096);                                       \
            if (ct == 3) LDS_B128(N.a[3], a_, 6144);                                       \
            SB0();                                                                         \
            bf16x8 b_ = cat8v(C.bl[ct], C.bh[ct]);                                         \
            _Pragma("unroll") for (int rt = 0; rt < 4; ++rt)                               \
                acc[rt][ct] = MFMA(b_, C.a[rt], acc[rt][ct]);                              \
            SB0();                                                                         \
        }                                                                                  \
    } while (0)
#define F4_READ(N, AADDR, SBO)                                                             \
    do {                                                                                   \
        const unsigned a_ = (AADDR), sb_ = (SBO);                                          \
        _Pragma("unroll") for (int ct = 0; ct < 4; ++ct) {                                 \
            LDS_TR(N.bl[ct], blo3[ct] + sb_, 0);  LDS_TR(N.bh[ct], blo3[ct] + sb_, 1024);  \
        }                                                                                  \
        LDS_B128(N.a[0], a_, 0); LDS_B128(N.a[1], a_, 2048); LDS_B128(N.a[2], a_, 4096); LDS_B128(N.a[3], a_, 6144); \
    } while (0)
#define F4_MMA(C)                                                                          \
    do {                                                                                   \
        _Pragma("unroll") for (int ct = 0; ct < 4; ++ct) {                                 \
            bf16x8 b_ = cat8v(C.bl[ct], C.bh[ct]);                                         \
            _Pragma("unroll") for (int rt = 0; rt < 4; ++rt)                               \
                acc[rt][ct] = MFMA(b_, C.a[rt], acc[rt][ct]);                              \
        }                                                                                  \
    } while (0)
#define F4_WAIT(F)                                                                                            \
    asm volatile("s_waitcnt lgkmcnt(0)"                                                                       \
                 : "+v"(F.a[0]), "+v"(F.a[1]), "+v"(F.a[2]), "+v"(F.a[3]), "+v"(F.bl[0]), "+v"(F.bl[1]),       \
                   "+v"(F.bl[2]), "+v"(F.bl[3]), "+v"(F.bh[0]), "+v"(F.bh[1]), "+v"(F.bh[2]), "+v"(F.bh[3]))

__global__ __launch_bounds__(768, 3) void k3_gate_up(const u16* __restrict__ x, const float2* __restrict__ pstats,
                                                     const u16* __restrict__ wg,
                                                     const float* __restrict__ csum, u16* __restrict__ hbuf) {
    extern __shared__ __attribute__((aligned(16))) char smem[];
    const int tid = threadIdx.x, lane = tid & 63, wave = __builtin_amdgcn_readfirstlane(tid >> 6);
    const int g = lane >> 4, l15 = lane & 15;
    const int rb = blockIdx.x & 7, cb = blockIdx.x >> 3;
    const int row0 = rb * 256, col0 = cb * 64;
    float2* stats = (float2*)(smem + K3_ST);
    STAMP(2, 0);
    if (wave >= 8) {
        const int lw = wave - 8;
        const char* srcA = (const char*)x + (size_t)(rb * 4 + lw) * 10 * 8192 + lane * 16;
        const char* srcB = (const char*)wg + (size_t)cb * 10 * 16384 + lw * 4096 + lane * 16;
        auto stage = [&](int s) {
            char* base = smem + (s % 3) * K3_STG;
#pragma unroll
            for (int i = 0; i < 4; ++i) glds16(srcB + (size_t)s * 16384 + i * 1024, base + 32768 + lw * 4096 + i * 1024);
#pragma unroll
            for (int i = 0; i < 8; ++i) glds16(srcA + (size_t)s * 8192 + i * 1024, base + lw * 8192 + i * 1024);
        };
        stage(0); stage(1);
#pragma unroll 1
        for (int b = 0; b < 10; ++b) {
            wait_stages<12, 1>(9 - b);
            BAR_ONLY();
            if (b + 2 < 10) stage(b + 2);
        }
        return;
    }
    __builtin_amdgcn_s_setprio(2);
    if (tid < 256) stats[tid] = combine_stats(pstats + (size_t)(row0 + tid) * 8);
    asm volatile("s_waitcnt lgkmcnt(0)" ::: "memory");
    const int wr = wave >> 1, wc = wave & 1;
    f32x4 acc[4][4];
#pragma unroll
    for (int i = 0; i < 4; ++i)
#pragma unroll
        for (int j = 0; j < 4; ++j) acc[i][j] = (f32x4){0.f, 0.f, 0.f, 0.f};
    const unsigned sm3 = lds_addr(smem);
    unsigned blo3[4];
    {
        int q = l15 >> 2, p4 = lane & 3;
        int sx = q | ((g & 1) << 2);
#pragma unroll
        for (int ct = 0; ct < 4; ++ct) {
            int nt = (ct >> 1) * 4 + wc * 2 + (ct & 1);
            blo3[ct] = sm3 + 32768 + (8 * g + q) * 256 + ((nt ^ sx) * 32) + p4 * 8;
        }
    }
    unsigned aad3[2];
    {
        int r = wr * 64 + l15;
        aad3[0] = sm3 + r * 128 + (((unsigned)(g ^ (r & 7))) << 4);
        aad3[1] = sm3 + r * 128 + (((unsigned)((4 + g) ^ (r & 7))) << 4);
    }
    Frag4x4 fa, fb;
    BAR_ONLY();
    STAMP(2, 1);
    F4_READ(fa, aad3[0], 0);
    F4_WAIT(fa);
#pragma unroll 1
    for (int s = 0; s < 9; ++s) {
        const unsigned so = (s % 3) * K3_STG;
        F4_STEP(fa, fb, aad3[1] + so, so + 8192);
        F4_WAIT(fb);
        BAR_ONLY();
        const unsigned sn = ((s + 1) % 3) * K3_STG;
        F4_STEP(fb, fa, aad3[0] + sn, sn);
        F4_WAIT(fa);
    }
    F4_STEP(fa, fb, aad3[1], 8192);
    F4_WAIT(fb);
    F4_MMA(fb);
    STAMP(2, 2);
    {
        f32x4 cs[4];
#pragma unroll
        for (int c2 = 0; c2 < 2; ++c2) {
            int col = col0 + (wc * 2 + c2) * 16 + 4 * g;
            cs[c2] = *(const f32x4*)(csum + col);
            cs[2 + c2] = *(const f32x4*)(csum + DF_ + col);
        }
#pragma unroll
        for (int rt = 0; rt < 4; ++rt) {
            int rl = wr * 64 + rt * 16 + l15;
            float2 st = stats[rl];
#pragma unroll
            for (int c2 = 0; c2 < 2; ++c2) {
                int col = col0 + (wc * 2 + c2) * 16 + 4 * g;
                float hv[4];
#pragma unroll
                for (int j = 0; j < 4; ++j) {
                    float gg = rbf((acc[rt][c2][j] - st.x * cs[c2][j]) * st.y);
                    float uu = rbf((acc[rt][2 + c2][j] - st.x * cs[2 + c2][j]) * st.y);
                    hv[j] = rbf(gelu_tanh(gg)) * uu;
                }
                u32x2 v;
                v[0] = pack2(hv[0], hv[1]);
                v[1] = pack2(hv[2], hv[3]);
                {
                    int row = row0 + rl, r = row & 63, cc = col & 63;
                    *(u32x2*)(hbuf + (size_t)((row >> 6) * 27 + cb) * 4096 + r * 64 + (((cc >> 3) ^ (r & 7)) << 3) + (cc & 7)) = v;
                }
            }
        }
    }
    STAMP(2, 3);
}

constexpr int K4_STG = 36864;
constexpr int K4_SMEM = 4 * K4_STG;

__global__ __launch_bounds__(768, 3) void k4_down(const u16* __restrict__ hbuf, const u16* __restrict__ wd,
                                                  const u16* __restrict__ xin, u16* __restrict__ xout,
                                                  u16* __restrict__ xtile, float2* __restrict__ stats) {
    extern __shared__ __attribute__((aligned(16))) char smem[];
    const int tid = threadIdx.x, lane = tid & 63, wave = __builtin_amdgcn_readfirstlane(tid >> 6);
    const int g = lane >> 4, l15 = lane & 15;
    const int rb = (blockIdx.x & 7) * 4 + ((blockIdx.x >> 3) & 3), cb = blockIdx.x >> 5;
    const int row0 = rb * 64, col0 = cb * 80;
    constexpr int NS = 14;
    STAMP(3, 0);

    if (wave >= 8) {
        const int lw = wave - 8;
        const char* srcA = (const char*)hbuf + (size_t)rb * 27 * 8192 + lw * 4096 + lane * 16;
        const char* srcB = (const char*)wd + (size_t)cb * 14 * 20480 + lw * 1024 + lane * 16;
        auto stage = [&](int s) {
            char* base = smem + (s & 3) * K4_STG;
            const int sa = (s == NS - 1 && lw >= 2) ? 2 * s - 1 : 2 * s;
#pragma unroll
            for (int i = 0; i < 4; ++i) glds16(srcA + (size_t)sa * 8192 + i * 1024, base + lw * 4096 + i * 1024);
#pragma unroll
            for (int i = 0; i < 5; ++i) glds16(srcB + (size_t)s * 20480 + i * 4096, base + 16384 + lw * 1024 + i * 4096);
        };
        stage(0); stage(1); stage(2);
#pragma unroll 1
        for (int b = 0; b < NS; ++b) {
            wait_stages<9, 2>(NS - 1 - b);
            BAR_ONLY();
            if (b + 3 < NS) stage(b + 3);
        }
        return;
    }
    __builtin_amdgcn_s_setprio(2);

    u32x2 xres[5];
    if (wave < 4) {
#pragma unroll
        for (int ct = 0; ct < 5; ++ct) xres[ct] = *(const u32x2*)(xin + (size_t)(row0 + wave * 16 + l15) * D_ + col0 + ct * 16 + 4 * g);
    }
    const int wr = wave & 1, kq = wave >> 1;
    f32x4 acc[2][5];
#pragma unroll
    for (int i = 0; i < 2; ++i)
#pragma unroll
        for (int j = 0; j < 5; ++j) acc[i][j] = (f32x4){0.f, 0.f, 0.f, 0.f};
    {
        const unsigned sm = lds_addr(smem);
        unsigned blo, bhi;
        wfrag_addr<160>(sm + 16384 + kq * 5120, lane, blo, bhi);
        const int r = wr * 32 + l15;
        const unsigned aad = sm + (kq >> 1) * 8192 + r * 128 + (((unsigned)(((kq & 1) * 4 + g) ^ (r & 7))) << 4);
        Frag2x5 fa, fb;
        BAR_ONLY();
        G5_READ(fa, aad, 2048, blo, bhi);
        G5_WAIT(fa);
#pragma unroll 1
        for (int s = 0; s < NS - 2; s += 2) {
            BAR_ONLY();
            { const unsigned so = ((s + 1) & 3) * K4_STG; G5_STEP(fa, fb, aad + so, 2048, blo + so, bhi + so); }
            G5_WAIT(fb);
            BAR_ONLY();
            { const unsigned so = ((s + 2) & 3) * K4_STG; G5_STEP(fb, fa, aad + so, 2048, blo + so, bhi + so); }
            G5_WAIT(fa);
        }
        BAR_ONLY();
        { const unsigned so = ((NS - 1) & 3) * K4_STG; G5_STEP(fa, fb, aad + so, 2048, blo + so, bhi + so); }
        G5_WAIT(fb);
        if (kq < 2) G5_MMA(fb);
    }
    __syncthreads();
    float* red = (float*)smem;
#pragma unroll
    for (int rt = 0; rt < 2; ++rt)
#pragma unroll
        for (int ct = 0; ct < 5; ++ct) *(f32x4*)(red + (((kq * 4 + wr * 2 + rt) * 5 + ct) * 64 + lane) * 4) = acc[rt][ct];
    __syncthreads();
    STAMP(3, 1);
    if (wave < 4) {
        const int row = row0 + wave * 16 + l15;
        float vals[20];
#pragma unroll
        for (int ct = 0; ct < 5; ++ct) {
            f32x4 o = *(const f32x4*)(red + ((wave * 5 + ct) * 64 + lane) * 4);
#pragma unroll
            for (int q = 1; q < 4; ++q) o += *(const f32x4*)(red + (((q * 4 + wave) * 5 + ct) * 64 + lane) * 4);
            int col = col0 + ct * 16 + 4 * g;
            vals[ct * 4 + 0] = rbf(lo16(xres[ct][0]) + rbf(o[0]));
            vals[ct * 4 + 1] = rbf(hi16(xres[ct][0]) + rbf(o[1]));
            vals[ct * 4 + 2] = rbf(lo16(xres[ct][1]) + rbf(o[2]));
            vals[ct * 4 + 3] = rbf(hi16(xres[ct][1]) + rbf(o[3]));
            u32x2 v;
            v[0] = pack2(vals[ct * 4 + 0], vals[ct * 4 + 1]);
            v[1] = pack2(vals[ct * 4 + 2], vals[ct * 4 + 3]);
            *(u32x2*)(xout + (size_t)row * D_ + col) = v;
            if (xtile) {
                int r = row & 63, cc = col & 63;
                *(u32x2*)(xtile + (size_t)(rb * 10 + (col >> 6)) * 4096 + r * 64 + (((cc >> 3) ^ (r & 7)) << 3) + (cc & 7)) = v;
            }
        }
        if (xtile) slice_stats(vals, stats + (size_t)row * 8 + cb, g);
    }
    STAMP(3, 2);
}

extern "C" void kernel_launch(void* const* d_in, const int* in_sizes, int n_in,
                              void* d_out, int out_size, void* d_ws, size_t ws_size,
                              hipStream_t stream) {
    const u16* A_x = (const u16*)d_in[0];
    const u16* wqkv = (const u16*)d_in[1];
    const u16* wphi = (const u16*)d_in[2];
    const u16* wo = (const u16*)d_in[3];
    const u16* wg = (const u16*)d_in[5];
    const u16* wu = (const u16*)d_in[6];
    const u16* wd = (const u16*)d_in[7];
    u16* out = (u16*)d_out;

    char* ws = (char*)d_ws;
    u16* x1 = (u16*)(ws);
    u16* x2 = (u16*)(ws + (4u << 20));
    char* phiq = ws + (8u << 20);
    float* kvp = (float*)(ws + (16u << 20));
    u16* kvt = (u16*)(ws + (24u << 20));
    float2* stats = (float2*)(ws + (25u << 20));
    float* csum = (float*)(ws + (26u << 20));
    float2* stats0 = (float2*)(ws + (27u << 20));
    u16* hbuf = (u16*)(ws + (28u << 20));
    u16* x1t = (u16*)(ws + (36u << 20));
    u16* x0t = (u16*)(ws + (52u << 20));
    char* wt = ws + (40u << 20);
    const u16* wq_t = (const u16*)wt;
    const u16* wo_t = (const u16*)(wt + (size_t)WQ_CH * 16);
    const u16* wgu_t = (const u16*)(wt + (size_t)(WQ_CH + WO_CH) * 16);
    const u16* wd_t = (const u16*)(wt + (size_t)(WQ_CH + WO_CH + WGU_CH) * 16);

    (void)hipFuncSetAttribute((const void*)k1_qkv, hipFuncAttributeMaxDynamicSharedMemorySize, K1_SMEM);
    (void)hipFuncSetAttribute((const void*)k2_attn_out, hipFuncAttributeMaxDynamicSharedMemorySize, K2_SMEM);
    (void)hipFuncSetAttribute((const void*)k3_gate_up, hipFuncAttributeMaxDynamicSharedMemorySize, K3_SMEM);
    (void)hipFuncSetAttribute((const void*)k4_down, hipFuncAttributeMaxDynamicSharedMemorySize, K4_SMEM);

    k0_prep<<<K0_XBLK + K0_CSBLK + K0_CPBLK, 256, 0, stream>>>(A_x, wqkv, wo, wg, wu, wd, (u32x4*)wt, csum, x0t, stats0);
    const u16* xin = A_x;
    for (int layer = 0; layer < 2; ++layer) {
        u16* xmid = x1;
        u16* xo = (layer == 0) ? x2 : out;
        k1_qkv<<<256, 768, K1_SMEM, stream>>>(x0t, stats0, wq_t, csum, wphi, phiq, kvp);
        kr_reduce<<<1280, 256, 0, stream>>>(kvp, kvt);
        k2_attn_out<<<256, 768, K2_SMEM, stream>>>(xin, phiq, kvt, wo_t, xmid, x1t, stats);
        k3_gate_up<<<216, 768, K3_SMEM, stream>>>(x1t, stats, wgu_t, csum, hbuf);
        k4_down<<<256, 768, K4_SMEM, stream>>>(hbuf, wd_t, xmid, xo, layer == 0 ? x0t : (u16*)nullptr, stats0);
        xin = x2;
    }
}
```

```cpp
#include <hip/hip_runtime.h>
#include <hip/hip_bf16.h>
#include <stdint.h>

constexpr int S_ = 2048, D_ = 640, H_ = 32, DH_ = 20, DF_ = 1728;
constexpr int N3_ = 3 * D_;

typedef __attribute__((ext_vector_type(8))) short bf16x8;
typedef __attribute__((ext_vector_type(4))) short bf16x4;
typedef __attribute__((ext_vector_type(4))) float f32x4;
typedef __attribute__((ext_vector_type(4))) unsigned int u32x4;
typedef __attribute__((ext_vector_type(2))) unsigned int u32x2;
typedef unsigned short u16;
typedef __attribute__((address_space(3))) bf16x4* lds_b4_ptr;
typedef __attribute__((address_space(3))) void* lds_vptr;
typedef const __attribute__((address_space(1))) void* glb_vptr;

__device__ __forceinline__ float bf2f(u16 v) { return __uint_as_float(((unsigned)v) << 16); }
__device__ __forceinline__ u16 f2bf(float f) { return __builtin_bit_cast(u16, (__bf16)f); }
__device__ __forceinline__ float rbf(float f) { return bf2f(f2bf(f)); }
__device__ __forceinline__ unsigned pack2(float a, float b) { return (unsigned)f2bf(a) | ((unsigned)f2bf(b) << 16); }
__device__ __forceinline__ float lo16(unsigned u) { return __uint_as_float(u << 16); }
__device__ __forceinline__ float hi16(unsigned u) { return __uint_as_float(u & 0xffff0000u); }
__device__ __forceinline__ bf16x4 tr_read(const char* p) { return __builtin_amdgcn_ds_read_tr16_b64_v4i16((lds_b4_ptr)p); }
__device__ __forceinline__ bf16x8 cat8v(bf16x4 a, bf16x4 b) { return __builtin_shufflevector(a, b, 0, 1, 2, 3, 4, 5, 6, 7); }
#define MFMA(a, b, c) __builtin_amdgcn_mfma_f32_16x16x32_bf16((a), (b), (c), 0, 0, 0)

template <int CTRL> __device__ __forceinline__ float dpp_mov(float x) {
    return __int_as_float(__builtin_amdgcn_update_dpp(0, __float_as_int(x), CTRL, 0xf, 0xf, false));
}
__device__ __forceinline__ float sum16(float x) {
    x += dpp_mov<0x128>(x); x += dpp_mov<0x124>(x); x += dpp_mov<0x122>(x); x += dpp_mov<0x121>(x);
    return x;
}
__device__ __forceinline__ void swap16(float x, float& a, float& b) {
    auto r = __builtin_amdgcn_permlane16_swap(__float_as_uint(x), __float_as_uint(x), false, false);
    a = __uint_as_float(r[0]); b = __uint_as_float(r[1]);
}
__device__ __forceinline__ void swap32(float x, float& a, float& b) {
    auto r = __builtin_amdgcn_permlane32_swap(__float_as_uint(x), __float_as_uint(x), false, false);
    a = __uint_as_float(r[0]); b = __uint_as_float(r[1]);
}
__device__ __forceinline__ float sum_g(float x) { float a, b; swap16(x, a, b); x = a + b; swap32(x, a, b); return a + b; }
__device__ __forceinline__ float max_g(float x) { float a, b; swap16(x, a, b); x = fmaxf(a, b); swap32(x, a, b); return fmaxf(a, b); }
__device__ __forceinline__ float min_g(float x) { float a, b; swap16(x, a, b); x = fminf(a, b); swap32(x, a, b); return fminf(a, b); }

__device__ __forceinline__ void glds16(const void* src, char* lds_dst) {
    __builtin_amdgcn_global_load_lds((glb_vptr)src, (lds_vptr)lds_dst, 16, 0, 0);
}
#define LDS_TR(dst, addr, off) asm volatile("ds_read_b64_tr_b16 %0, %1 offset:%2" : "=v"(dst) : "v"(addr), "i"(off))
#define LDS_B128(dst, addr, off) asm volatile("ds_read_b128 %0, %1 offset:%2" : "=v"(dst) : "v"(addr), "i"(off))
__device__ __forceinline__ unsigned lds_addr(const void* p) { return (unsigned)(size_t)p; }
template <int RS>
__device__ __forceinline__ void wfrag_addr(unsigned img, int lane, unsigned& lo, unsigned& hi) {
    int g = lane >> 4, q = (lane & 15) >> 2, p = lane & 3;
    int sw = (g & 1) * 4;
    lo = img + (8 * g + sw + q) * RS + p * 8;
    hi = img + (8 * g + (4 ^ sw) + q) * RS + p * 8;
}
template <int N> __device__ __forceinline__ void wait_vm() { asm volatile("s_waitcnt vmcnt(%0)" ::"n"(N) : "memory"); }
template <int PER, int MAXS>
__device__ __forceinline__ void wait_stages(int stages) {
    if (stages >= MAXS) { wait_vm<PER * MAXS>(); return; }
    if constexpr (MAXS > 0) wait_stages<PER, MAXS - 1>(stages);
}

#define BAR_ONLY() do { asm volatile("" ::: "memory"); __builtin_amdgcn_s_barrier(); asm volatile("" ::: "memory"); } while (0)
#define SB0() __builtin_amdgcn_sched_barrier(0)

constexpr int PHT_ = 1280;
constexpr int KVT_H = 21 * 40;

constexpr int WQ_CH = 8 * 10 * 1920;
constexpr int WO_CH = 8 * 5 * 1280;
constexpr int WGU_CH = 27 * 10 * 1024;
constexpr int WD_CH = 8 * 14 * 1280;
constexpr int K0_CH = WQ_CH + WO_CH + WGU_CH + WD_CH;
constexpr int K0_CPBLK = (K0_CH + 1023) / 1024;
constexpr int K0_CSBLK = 54 + 30;
constexpr int K0_XBLK = 128;

__device__ __forceinline__ const u16* k0_src(int cc, const u16* wqkv, const u16* wo, const u16* wg, const u16* wu,
                                             const u16* wd, bool& zero) {
    zero = false;
    if (cc < WQ_CH) {
        int blk = cc / 1920, r = cc - blk * 1920;
        int hg = blk / 10, st = blk - hg * 10;
        int pos = r / 30, nch = r - pos * 30;
        int k = pos ^ (((pos >> 3) & 1) << 2);
        int sec = nch / 10, w = nch - sec * 10;
        return wqkv + (size_t)(st * 64 + k) * N3_ + sec * D_ + hg * 80 + w * 8;
    }
    cc -= WQ_CH;
    if (cc < WO_CH) {
        int blk = cc / 1280, r = cc - blk * 1280;
        int cb = blk / 5, st = blk - cb * 5;
        int pos = r / 10, nch = r - pos * 10;
        int k = pos ^ (((pos >> 3) & 1) << 2);
        return wo + (size_t)(st * 128 + k) * D_ + cb * 80 + nch * 8;
    }
    cc -= WO_CH;
    if (cc < WGU_CH) {
        int blk = cc >> 10, r = cc & 1023;
        int cb = blk / 10, st = blk - cb * 10;
        int k = r >> 4, cpos = r & 15;
        int sx = (k & 3) | (((k >> 3) & 1) << 2);
        int nch = (((cpos >> 1) ^ sx) << 1) | (cpos & 1);
        const u16* w = (nch < 8) ? wg : wu;
        return w + (size_t)(st * 64 + k) * DF_ + cb * 64 + (nch & 7) * 8;
    }
    cc -= WGU_CH;
    int blk = cc / 1280, r = cc - blk * 1280;
    int cb = blk / 14, st = blk - cb * 14;
    int pos = r / 10, nch = r - pos * 10;
    int k = st * 128 + (pos ^ (((pos >> 3) & 1) << 2));
    zero = (k >= DF_);
    if (zero) k = 0;
    return wd + (size_t)k * D_ + cb * 80 + nch * 8;
}

__global__ __launch_bounds__(256) void k0_prep(const u16* __restrict__ x, const u16* __restrict__ wqkv,
                                               const u16* __restrict__ wo, const u16* __restrict__ wg,
                                               const u16* __restrict__ wu, const u16* __restrict__ wd,
                                               u32x4* __restrict__ wt, float* __restrict__ csum,
                                               u16* __restrict__ xtile, float2* __restrict__ stats) {
    __shared__ float red[2048];
    const int tid = threadIdx.x, bid = blockIdx.x;
    if (bid < K0_XBLK) {
        const int lane = tid & 63, g = lane >> 4, l15 = lane & 15;
        const int row = bid * 16 + (tid >> 6) * 4 + g;
        u32x4 c[5];
#pragma unroll
        for (int i = 0; i < 5; ++i) c[i] = *(const u32x4*)(x + (size_t)row * D_ + (l15 + 16 * i) * 8);
        float sm = 0.f;
#pragma unroll
        for (int i = 0; i < 5; ++i)
#pragma unroll
            for (int j = 0; j < 4; ++j) { sm += lo16(c[i][j]); sm += hi16(c[i][j]); }
        sm = sum16(sm);
        float mean = sm * (1.0f / 640.0f), q = 0.f;
#pragma unroll
        for (int i = 0; i < 5; ++i)
#pragma unroll
            for (int j = 0; j < 4; ++j) {
                float a = lo16(c[i][j]) - mean, b = hi16(c[i][j]) - mean;
                q += a * a; q += b * b;
            }
        q = sum16(q);
        if (l15 < 8) stats[(size_t)row * 8 + l15] = make_float2(mean, q * 0.125f);
        const int r = row & 63;
#pragma unroll
        for (int i = 0; i < 5; ++i) {
            int ch = l15 + 16 * i;
            *(u32x4*)(xtile + (size_t)((row >> 6) * 10 + (ch >> 3)) * 4096 + r * 64 + (((ch & 7) ^ (r & 7)) << 3)) = c[i];
        }
    } else if (bid < K0_XBLK + K0_CSBLK) {
        const int cbk = bid - K0_XBLK;
        const u16* w; int ld, col0, obase;
        if (cbk < 54) { w = (cbk < 27) ? wg : wu; ld = DF_; col0 = (cbk % 27) * 64; obase = (cbk < 27 ? 0 : DF_) + col0; }
        else { w = wqkv; ld = N3_; col0 = (cbk - 54) * 64; obase = 2 * DF_ + col0; }
        const int c8 = tid & 7, rg = tid >> 3;
        float s[8] = {0.f, 0.f, 0.f, 0.f, 0.f, 0.f, 0.f, 0.f};
        u32x4 v[20];
#pragma unroll
        for (int i = 0; i < 20; ++i) v[i] = *(const u32x4*)(w + (size_t)(rg * 20 + i) * ld + col0 + c8 * 8);
#pragma unroll
        for (int i = 0; i < 20; ++i)
#pragma unroll
            for (int j = 0; j < 4; ++j) { s[2 * j] += lo16(v[i][j]); s[2 * j + 1] += hi16(v[i][j]); }
#pragma unroll
        for (int j = 0; j < 8; ++j) red[rg * 64 + c8 * 8 + j] = s[j];
        __syncthreads();
        if (tid < 64) {
            float t = 0.f;
#pragma unroll
            for (int i = 0; i < 32; ++i) t += red[i * 64 + tid];
            csum[obase + tid] = t;
        }
    } else {
        const int c0 = (bid - K0_XBLK - K0_CSBLK) * 1024 + tid;
        u32x4 v[4];
        bool z[4];
#pragma unroll
        for (int i = 0; i < 4; ++i) {
            int c = c0 + 256 * i; if (c >= K0_CH) c = K0_CH - 1;
            v[i] = *(const u32x4*)k0_src(c, wqkv, wo, wg, wu, wd, z[i]);
        }
#pragma unroll
        for (int i = 0; i < 4; ++i) {
            int c = c0 + 256 * i;
            if (c < K0_CH) wt[c] = z[i] ? (u32x4){0u, 0u, 0u, 0u} : v[i];
        }
    }
}

__device__ __forceinline__ float2 combine_stats(const float2* ps8) {
    const f32x4* ps = (const f32x4*)ps8;
    f32x4 p0 = ps[0], p1 = ps[1], p2 = ps[2], p3 = ps[3];
    float mean = (p0[0] + p0[2] + p1[0] + p1[2] + p2[0] + p2[2] + p3[0] + p3[2]) * 0.125f;
    float m2 = p0[1] + p0[3] + p1[1] + p1[3] + p2[1] + p2[3] + p3[1] + p3[3];
    float dm = 0.f, t;
    t = p0[0] - mean; dm += t * t; t = p0[2] - mean; dm += t * t;
    t = p1[0] - mean; dm += t * t; t = p1[2] - mean; dm += t * t;
    t = p2[0] - mean; dm += t * t; t = p2[2] - mean; dm += t * t;
    t = p3[0] - mean; dm += t * t; t = p3[2] - mean; dm += t * t;
    float var_bf = rbf((m2 + 80.0f * dm) * (1.0f / 640.0f));
    float den = rbf(sqrtf(rbf(var_bf + 1e-5f)));
    return make_float2(rbf(mean), 1.0f / den);
}

constexpr int K1_STG = 38912;
constexpr int K1_WP = 65536;
constexpr int K1_ST = 4 * K1_STG;
constexpr int K1_SMEM = K1_ST + 512;
constexpr int K1_T = 0;
constexpr int K1_TRS = 144;
constexpr int K1_PK = 256 * K1_TRS;
constexpr int K1_PKRS = 336;

struct Frag2x4 { bf16x8 a[2]; bf16x4 bl[4], bh[4]; };
#define K1_READ_CT(N, ct, so) do { LDS_TR(N.bl[ct], blo + (so), ct * 32); LDS_TR(N.bh[ct], bhi + (so), ct * 32); } while (0)
#define K1_WAIT(F)                                                                                      \
    asm volatile("s_waitcnt lgkmcnt(0)"                                                                 \
                 : "+v"(F.a[0]), "+v"(F.a[1]), "+v"(F.bl[0]), "+v"(F.bl[1]), "+v"(F.bl[2]), "+v"(F.bl[3]), \
                   "+v"(F.bh[0]), "+v"(F.bh[1]), "+v"(F.bh[2]), "+v"(F.bh[3]))
#define K1_READ(N, AA, SO)                                                                              \
    do {                                                                                                \
        const unsigned aa_ = (AA), so_ = (SO);                                                          \
        LDS_B128(N.a[0], aa_, 0); LDS_B128(N.a[1], aa_, 2048);                                          \
        K1_READ_CT(N, 0, so_); K1_READ_CT(N, 1, so_); K1_READ_CT(N, 2, so_); K1_READ_CT(N, 3, so_);     \
    } while (0)
#define K1_MMA_CT(C, ct)                                                                                \
    do {                                                                                                \
        bf16x8 b_ = cat8v(C.bl[ct], C.bh[ct]);                                                          \
        acc[0][ct] = MFMA(C.a[0], b_, acc[0][ct]);                                                      \
        acc[1][ct] = MFMA(C.a[1], b_, acc[1][ct]);                                                      \
    } while (0)
#define K1_STEP(C, N, AA, SO)                                                                           \
    do {                                                                                                \
        const unsigned aa_ = (AA), so_ = (SO);                                                          \
        LDS_B128(N.a[0], aa_, 0); K1_READ_CT(N, 0, so_); SB0(); K1_MMA_CT(C, 0); SB0();                 \
        LDS_B128(N.a[1], aa_, 2048); K1_READ_CT(N, 1, so_); SB0(); K1_MMA_CT(C, 1); SB0();              \
        K1_READ_CT(N, 2, so_); SB0(); K1_MMA_CT(C, 2); SB0();                                           \
        K1_READ_CT(N, 3, so_); SB0(); K1_MMA_CT(C, 3); SB0();                                           \
    } while (0)

__global__ __launch_bounds__(768, 3) void k1_qkv(const u16* __restrict__ xt, const float2* __restrict__ pstats,
                                                 const u16* __restrict__ wqkv, const float* __restrict__ csum,
                                                 const u16* __restrict__ wphi, char* __restrict__ phiq,
                                                 float* __restrict__ kvp) {
    extern __shared__ __attribute__((aligned(16))) char smem[];
    const int tid = threadIdx.x, lane = tid & 63, wave = __builtin_amdgcn_readfirstlane(tid >> 6);
    const int g = lane >> 4, l15 = lane & 15;
    const int hg = blockIdx.x & 7, rb = blockIdx.x >> 3;
    const int row0 = rb * 64;
    float2* stats = (float2*)(smem + K1_ST);

    if (wave >= 8) {
        const int lw = wave - 8;
        const char* srcA = (const char*)xt + (size_t)rb * 10 * 8192 + lw * 2048 + lane * 16;
        const char* srcB = (const char*)wqkv + (size_t)hg * 10 * 30720 + lw * 1024 + lane * 16;
        auto stage = [&](int s) {
            char* base = smem + (s & 3) * K1_STG;
#pragma unroll
            for (int i = 0; i < 2; ++i) glds16(srcA + (size_t)s * 8192 + i * 1024, base + lw * 2048 + i * 1024);
#pragma unroll
            for (int i = 0; i < 7; ++i) glds16(srcB + (size_t)s * 30720 + i * 4096, base + 8192 + lw * 1024 + i * 4096);
            if (lw < 2) glds16(srcB + (size_t)s * 30720 + 7 * 4096, base + 8192 + lw * 1024 + 7 * 4096);
        };
        stage(0); stage(1); stage(2);
#pragma unroll 1
        for (int b = 0; b < 10; ++b) {
            if (lw < 2) wait_stages<10, 2>(9 - b); else wait_stages<9, 2>(9 - b);
            BAR_ONLY();
            if (b + 3 < 10) stage(b + 3);
        }
        return;
    }

    if (tid < 64) stats[tid] = combine_stats(pstats + (size_t)(row0 + tid) * 8);
    bf16x8 wpfrag;
    {
        int hh = tid >> 7, t = (tid >> 6) & 1;
        int wf = (t == 0) ? (5 * (l15 >> 2) + (l15 & 3)) : (((l15 & 3) == 0) ? (5 * (l15 >> 2) + 4) : -1);
        int fc = wf < 0 ? 0 : wf;
        u16 wpv[8];
#pragma unroll
        for (int j = 0; j < 8; ++j) {
            int d = 8 * g + j; if (d > 19) d = 19;
            wpv[j] = wphi[((hg * 4 + hh) * DH_ + d) * DH_ + fc];
        }
        bf16x8 v;
#pragma unroll
        for (int j = 0; j < 8; ++j) v[j] = (wf >= 0 && 8 * g + j < DH_) ? (short)wpv[j] : (short)0;
        wpfrag = v;
    }
    asm volatile("s_waitcnt lgkmcnt(0)" ::: "memory");

    const int wr = wave >> 2, wc = wave & 3;
    f32x4 acc[2][4];
#pragma unroll
    for (int i = 0; i < 2; ++i)
#pragma unroll
        for (int j = 0; j < 4; ++j) acc[i][j] = (f32x4){0.f, 0.f, 0.f, 0.f};
    const int nct = (wc == 3) ? 3 : 4;
    {
        const unsigned sm = lds_addr(smem);
        unsigned blo, bhi;
        wfrag_addr<480>(sm + 8192 + wc * 4 * 32, lane, blo, bhi);
        const int r = wr * 32 + l15;
        const unsigned aad0 = sm + r * 128 + (((unsigned)(g ^ (r & 7))) << 4);
        const unsigned aad1 = sm + r * 128 + (((unsigned)((4 + g) ^ (r & 7))) << 4);
        Frag2x4 fa, fb;
        BAR_ONLY();
        K1_READ(fa, aad0, 0);
        K1_WAIT(fa);
#pragma unroll 1
        for (int s = 0; s < 9; ++s) {
            const unsigned so = (s & 3) * K1_STG;
            K1_STEP(fa, fb, aad1 + so, so + 32 * 480);
            K1_WAIT(fb);
            BAR_ONLY();
            const unsigned sn = ((s + 1) & 3) * K1_STG;
            K1_STEP(fb, fa, aad0 + sn, sn);
            K1_WAIT(fa);
        }
        K1_STEP(fa, fb, aad1 + K1_STG, K1_STG + 32 * 480);
        K1_WAIT(fb);
        K1_MMA_CT(fb, 0); K1_MMA_CT(fb, 1); K1_MMA_CT(fb, 2); K1_MMA_CT(fb, 3);
    }
    __syncthreads();

    *(bf16x8*)(smem + K1_WP + tid * 16) = wpfrag;
    {
        float2 st[2][4];
#pragma unroll
        for (int rt = 0; rt < 2; ++rt)
#pragma unroll
            for (int j = 0; j < 4; ++j) st[rt][j] = stats[wr * 32 + rt * 16 + 4 * g + j];
#pragma unroll
        for (int ct = 0; ct < 4; ++ct) {
            if (ct < nct) {
                int col = (wc * 4 + ct) * 16 + l15;
                int sec = col / 80;
                float cs = csum[2 * DF_ + sec * D_ + hg * 80 + (col - sec * 80)];
#pragma unroll
                for (int rt = 0; rt < 2; ++rt) {
                    float y[4];
#pragma unroll
                    for (int j = 0; j < 4; ++j) y[j] = (acc[rt][ct][j] - st[rt][j].x * cs) * st[rt][j].y;
                    u32x2 v;
                    v[0] = pack2(y[0], y[1]);
                    v[1] = pack2(y[2], y[3]);
                    *(u32x2*)(smem + K1_T + col * K1_TRS + (wr * 32 + rt * 16 + 4 * g) * 2) = v;
                }
            }
        }
    }
    {
        int c = 240 + (tid >> 5), w = tid & 31;
        *(unsigned*)(smem + K1_T + c * K1_TRS + w * 4) = (c == 240) ? 0x3f803f80u : 0u;
    }
    __syncthreads();

    {
        const int q4 = l15 >> 2, p4 = lane & 3;
        const int rowt = (wave >> 1) * 16, sec = wave & 1;
        float p[4][5];
#pragma unroll
        for (int hh = 0; hh < 4; ++hh) {
            bf16x8 wp0 = *(const bf16x8*)(smem + K1_WP + ((hh * 2 + 0) * 64 + lane) * 16);
            bf16x8 wp1 = *(const bf16x8*)(smem + K1_WP + ((hh * 2 + 1) * 64 + lane) * 16);
            int c0 = sec * 80 + hh * 20 + 8 * g + q4;
            const char* tb = smem + K1_T + c0 * K1_TRS + (rowt + 4 * p4) * 2;
            bf16x8 b = cat8v(tr_read(tb), tr_read(tb + 4 * K1_TRS));
            f32x4 z = {0.f, 0.f, 0.f, 0.f};
            f32x4 d0 = MFMA(wp0, b, z);
            f32x4 d1 = MFMA(wp1, b, z);
            p[hh][0] = rbf(d0[0]); p[hh][1] = rbf(d0[1]); p[hh][2] = rbf(d0[2]); p[hh][3] = rbf(d0[3]); p[hh][4] = rbf(d1[0]);
        }
#pragma unroll
        for (int hh = 0; hh < 4; ++hh) {
            float mx = fmaxf(fmaxf(fmaxf(p[hh][0], p[hh][1]), fmaxf(p[hh][2], p[hh][3])), p[hh][4]);
            float mn = fminf(fminf(fminf(p[hh][0], p[hh][1]), fminf(p[hh][2], p[hh][3])), p[hh][4]);
            mx = max_g(mx);
            mn = min_g(mn);
            float ep[5], en[5], sp = 0.f, sn = 0.f;
#pragma unroll
            for (int i = 0; i < 5; ++i) {
                ep[i] = __expf(p[hh][i] - mx); sp += ep[i];
                en[i] = __expf(mn - p[hh][i]); sn += en[i];
            }
            sp = sum_g(sp);
            sn = sum_g(sn);
            float ip = __builtin_amdgcn_rcpf(sp), in_ = __builtin_amdgcn_rcpf(sn);
            u32x4 o;
            o[0] = pack2(ep[0] * ip, ep[1] * ip);
            o[1] = pack2(ep[2] * ip, ep[3] * ip);
            o[2] = pack2(ep[4] * ip, en[0] * in_);
            o[3] = pack2(en[1] * in_, en[2] * in_);
            unsigned o4 = pack2(en[3] * in_, en[4] * in_);
            if (sec == 0) {
                char* dst = phiq + (size_t)(((row0 + rowt) >> 4) * H_ + hg * 4 + hh) * PHT_;
                *(u32x4*)(dst + lane * 16) = o;
                *(unsigned*)(dst + 1024 + l15 * 16 + g * 4) = o4;
            } else {
                char* dst = smem + K1_PK + (rowt + l15) * K1_PKRS + hh * 80;
                *(u32x4*)(dst + 16 * g) = o;
                *(unsigned*)(dst + 64 + 4 * g) = o4;
            }
        }
    }
    __syncthreads();

    {
        const int hh = wave >> 1, dt = wave & 1;
        const int q4 = l15 >> 2, p4 = lane & 3;
        f32x4 kacc[3];
#pragma unroll
        for (int ft = 0; ft < 3; ++ft) kacc[ft] = (f32x4){0.f, 0.f, 0.f, 0.f};
#pragma unroll
        for (int ks = 0; ks < 2; ++ks) {
            int c = 160 + hh * 20 + dt * 16 + l15;
            if (dt == 1 && l15 >= 4) c = 240;
            bf16x8 b = *(const bf16x8*)(smem + K1_T + c * K1_TRS + (32 * ks + 8 * g) * 2);
#pragma unroll
            for (int ft = 0; ft < 3; ++ft) {
                const char* pb = smem + K1_PK + (32 * ks + 8 * g + q4) * K1_PKRS + (hh * 40 + 16 * ft + 4 * p4) * 2;
                bf16x8 a = cat8v(tr_read(pb), tr_read(pb + 4 * K1_PKRS));
                kacc[ft] = MFMA(a, b, kacc[ft]);
            }
        }
        float* dst = kvp + (size_t)(rb * H_ + hg * 4 + hh) * 40 * 32;
#pragma unroll
        for (int ft = 0; ft < 3; ++ft)
#pragma unroll
            for (int j = 0; j < 4; ++j) {
                int f = 16 * ft + 4 * g + j;
                if (f < 40) dst[f * 32 + dt * 16 + l15] = kacc[ft][j];
            }
    }
}

__global__ __launch_bounds__(256) void kr_reduce(const float* __restrict__ kvp, u16* __restrict__ kvt) {
    __shared__ float red[128];
    const int tid = threadIdx.x;
    const int d = tid & 31, half = (tid >> 5) & 1, rr = tid >> 6;
    const int hf = blockIdx.x * 4 + rr;
    const int h = hf / 40, f = hf - h * 40;
    float v[16];
#pragma unroll
    for (int i = 0; i < 16; ++i) v[i] = kvp[((size_t)((half * 16 + i) * H_ + h) * 40 + f) * 32 + d];
    float s = 0.f;
#pragma unroll
    for (int i = 0; i < 16; ++i) s += v[i];
    if (half) red[rr * 32 + d] = s;
    __syncthreads();
    if (!half && d < 21) kvt[h * KVT_H + d * 40 + f] = f2bf(s + red[rr * 32 + d]);
}

struct Frag2x5 { bf16x8 a[2]; bf16x4 bl[5], bh[5]; };
#define G5_READ_CT(N, ct) do { LDS_TR(N.bl[ct], l_, ct * 32); LDS_TR(N.bh[ct], h_, ct * 32); } while (0)
#define G5_MMA_CT(C, ct)                                                                   \
    do {                                                                                   \
        bf16x8 b_ = cat8v(C.bl[ct], C.bh[ct]);                                             \
        acc[0][ct] = MFMA(b_, C.a[0], acc[0][ct]);                                         \
        acc[1][ct] = MFMA(b_, C.a[1], acc[1][ct]);                                         \
    } while (0)
#define G5_READ(N, AADDR, AOFF, BLO, BHI)                                                  \
    do {                                                                                   \
        const unsigned a_ = (AADDR), l_ = (BLO), h_ = (BHI);                               \
        LDS_B128(N.a[0], a_, 0); LDS_B128(N.a[1], a_, AOFF);                               \
        G5_READ_CT(N, 0); G5_READ_CT(N, 1); G5_READ_CT(N, 2); G5_READ_CT(N, 3); G5_READ_CT(N, 4); \
    } while (0)
#define G5_STEP(C, N, AADDR, AOFF, BLO, BHI)                                               \
    do {                                                                                   \
        const unsigned a_ = (AADDR), l_ = (BLO), h_ = (BHI);                               \
        LDS_B128(N.a[0], a_, 0); G5_READ_CT(N, 0); SB0(); G5_MMA_CT(C, 0); SB0();          \
        LDS_B128(N.a[1], a_, AOFF); G5_READ_CT(N, 1); SB0(); G5_MMA_CT(C, 1); SB0();       \
        G5_READ_CT(N, 2); SB0(); G5_MMA_CT(C, 2); SB0();                                   \
        G5_READ_CT(N, 3); SB0(); G5_MMA_CT(C, 3); SB0();                                   \
        G5_READ_CT(N, 4); SB0(); G5_MMA_CT(C, 4); SB0();                                   \
    } while (0)
#define G5_MMA(C) do { G5_MMA_CT(C, 0); G5_MMA_CT(C, 1); G5_MMA_CT(C, 2); G5_MMA_CT(C, 3); G5_MMA_CT(C, 4); } while (0)
#define G5_WAIT(F)                                                                                         \
    asm volatile("s_waitcnt lgkmcnt(0)"                                                                    \
                 : "+v"(F.a[0]), "+v"(F.a[1]), "+v"(F.bl[0]), "+v"(F.bl[1]), "+v"(F.bl[2]), "+v"(F.bl[3]),  \
                   "+v"(F.bl[4]), "+v"(F.bh[0]), "+v"(F.bh[1]), "+v"(F.bh[2]), "+v"(F.bh[3]), "+v"(F.bh[4]))

__device__ __forceinline__ void slice_stats(const float (&v)[20], float2* dst, int g) {
    float s = 0.f;
#pragma unroll
    for (int i = 0; i < 20; ++i) s += v[i];
    s = sum_g(s);
    float m = s * (1.0f / 80.0f);
    float q = 0.f;
#pragma unroll
    for (int i = 0; i < 20; ++i) { float t = v[i] - m; q += t * t; }
    q = sum_g(q);
    if (g == 0) *dst = make_float2(m, q);
}

constexpr int K2_O = 0;
constexpr int K2_B = 40960;
constexpr int K2_STG = 40960;
constexpr int K2_SMEM = K2_B + 3 * K2_STG;

__global__ __launch_bounds__(768, 3) void k2_attn_out(const u16* __restrict__ x, const char* __restrict__ phiq,
                                                      const u16* __restrict__ kvt, const u16* __restrict__ wo,
                                                      u16* __restrict__ xout, u16* __restrict__ xtile,
                                                      float2* __restrict__ stats) {
    extern __shared__ __attribute__((aligned(16))) char smem[];
    const int tid = threadIdx.x, lane = tid & 63, wave = __builtin_amdgcn_readfirstlane(tid >> 6);
    const int g = lane >> 4, l15 = lane & 15;
    const int rb = (blockIdx.x & 7) * 8 + ((blockIdx.x >> 3) & 7), cp = blockIdx.x >> 6;
    const int row0 = rb * 32;

    if (wave >= 8) {
        const int lw = wave - 8;
        const char* src = (const char*)wo + (size_t)(cp * 2 + (lw >> 1)) * 5 * 20480 + (lw & 1) * 10240 + lane * 16;
        auto stage = [&](int s) {
            char* base = smem + K2_B + (s % 3) * K2_STG + lw * 10240;
#pragma unroll
            for (int i = 0; i < 10; ++i) glds16(src + (size_t)s * 20480 + i * 1024, base + i * 1024);
        };
        stage(0); stage(1); stage(2);
#pragma unroll 1
        for (int b = 0; b < 5; ++b) {
            wait_stages<10, 1>(4 - b);
            BAR_ONLY();
            if (b >= 1 && b + 2 < 5) stage(b + 2);
        }
        return;
    }

    u32x2 xres[5];
    if (wave < 4) {
#pragma unroll
        for (int ct = 0; ct < 5; ++ct)
            xres[ct] = *(const u32x2*)(x + (size_t)(row0 + (wave & 1) * 16 + l15) * D_ + (cp * 2 + (wave >> 1)) * 80 + ct * 16 + 4 * g);
    }
    {
        struct HF { bf16x8 a[2][2], b0[2], b1[2]; };
        auto load_head = [&](HF& F, int h) {
#pragma unroll
            for (int rt = 0; rt < 2; ++rt) {
                const char* pr = phiq + (size_t)((rb * 2 + rt) * H_ + h) * PHT_;
                F.b0[rt] = *(const bf16x8*)(pr + lane * 16);
                F.b1[rt] = *(const bf16x8*)(pr + 1024 + l15 * 16);
            }
#pragma unroll
            for (int dt = 0; dt < 2; ++dt) {
                int d = 16 * dt + l15; if (d > 20) d = 20;
                F.a[dt][0] = *(const bf16x8*)(kvt + h * KVT_H + d * 40 + 8 * g);
                F.a[dt][1] = *(const bf16x8*)(kvt + h * KVT_H + d * 40 + 32);
            }
        };
        auto do_head = [&](const HF& F, int h) {
            f32x4 d0[2], d1[2];
            const f32x4 z = {0.f, 0.f, 0.f, 0.f};
#pragma unroll
            for (int rt = 0; rt < 2; ++rt) { d0[rt] = MFMA(F.a[0][0], F.b0[rt], z); d1[rt] = MFMA(F.a[1][0], F.b0[rt], z); }
#pragma unroll
            for (int rt = 0; rt < 2; ++rt) {
                bf16x8 bb1 = F.b1[rt];
                if (g != 0) bb1 = (bf16x8){0, 0, 0, 0, 0, 0, 0, 0};
                d0[rt] = MFMA(F.a[0][1], bb1, d0[rt]);
                d1[rt] = MFMA(F.a[1][1], bb1, d1[rt]);
            }
            float rd[2];
#pragma unroll
            for (int rt = 0; rt < 2; ++rt) rd[rt] = sum_g((g == 1) ? d1[rt][0] : 0.f);
#pragma unroll
            for (int rt = 0; rt < 2; ++rt) rd[rt] = __builtin_amdgcn_rcpf(rbf(rbf(rd[rt]) + 1e-6f));
            const int bo = (h * 20 + 4 * g) * 2, bo2 = (h * 20 + 16) * 2;
#pragma unroll
            for (int rt = 0; rt < 2; ++rt) {
                int r = rt * 16 + l15;
                u32x2 v;
                v[0] = pack2(rbf(d0[rt][0]) * rd[rt], rbf(d0[rt][1]) * rd[rt]);
                v[1] = pack2(rbf(d0[rt][2]) * rd[rt], rbf(d0[rt][3]) * rd[rt]);
                *(u32x2*)(smem + K2_O + r * 1280 + (((bo >> 4) ^ (r & 15)) << 4) + (bo & 15)) = v;
                if (g == 0) {
                    v[0] = pack2(rbf(d1[rt][0]) * rd[rt], rbf(d1[rt][1]) * rd[rt]);
                    v[1] = pack2(rbf(d1[rt][2]) * rd[rt], rbf(d1[rt][3]) * rd[rt]);
                    *(u32x2*)(smem + K2_O + r * 1280 + (((bo2 >> 4) ^ (r & 15)) << 4) + (bo2 & 15)) = v;
                }
            }
        };
        HF f0, f1, f2, f3;
        const int h0 = wave * 4;
        load_head(f0, h0);
        load_head(f1, h0 + 1);
        load_head(f2, h0 + 2);
        load_head(f3, h0 + 3);
        do_head(f0, h0);
        do_head(f1, h0 + 1);
        do_head(f2, h0 + 2);
        do_head(f3, h0 + 3);
    }
    asm volatile("s_waitcnt lgkmcnt(0)" ::: "memory");

    const int ch = wave & 1, kq = wave >> 1;
    f32x4 acc[2][5];
#pragma unroll
    for (int i = 0; i < 2; ++i)
#pragma unroll
        for (int j = 0; j < 5; ++j) acc[i][j] = (f32x4){0.f, 0.f, 0.f, 0.f};
    {
        const unsigned sm = lds_addr(smem);
        unsigned blo, bhi;
        wfrag_addr<160>(sm + K2_B + ch * 20480 + kq * 5120, lane, blo, bhi);
        const unsigned aad = sm + K2_O + l15 * 1280;
#define K2_AA(s) (aad + (((unsigned)(((s) * 16 + kq * 4 + g) ^ l15)) << 4))
        Frag2x5 fa, fb;
        BAR_ONLY();
        G5_READ(fa, K2_AA(0), 16 * 1280, blo, bhi);
        G5_WAIT(fa);
        BAR_ONLY();
        G5_STEP(fa, fb, K2_AA(1), 16 * 1280, blo + K2_STG, bhi + K2_STG);
        G5_WAIT(fb);
        BAR_ONLY();
        G5_STEP(fb, fa, K2_AA(2), 16 * 1280, blo + 2 * K2_STG, bhi + 2 * K2_STG);
        G5_WAIT(fa);
        BAR_ONLY();
        G5_STEP(fa, fb, K2_AA(3), 16 * 1280, blo, bhi);
        G5_WAIT(fb);
        BAR_ONLY();
        G5_STEP(fb, fa, K2_AA(4), 16 * 1280, blo + K2_STG, bhi + K2_STG);
        G5_WAIT(fa);
        G5_MMA(fa);
    }
    __syncthreads();
    float* red = (float*)smem;
#pragma unroll
    for (int rt = 0; rt < 2; ++rt)
#pragma unroll
        for (int ct = 0; ct < 5; ++ct) *(f32x4*)(red + (((kq * 4 + ch * 2 + rt) * 5 + ct) * 64 + lane) * 4) = acc[rt][ct];
    __syncthreads();
    if (wave < 4) {
        const int rt = wave & 1, hc = wave >> 1;
        const int row = row0 + rt * 16 + l15;
        const int cb = cp * 2 + hc, col0 = cb * 80;
        const int slot = hc * 2 + rt;
        float vals[20];
#pragma unroll
        for (int ct = 0; ct < 5; ++ct) {
            f32x4 o = *(const f32x4*)(red + ((slot * 5 + ct) * 64 + lane) * 4);
#pragma unroll
            for (int q = 1; q < 4; ++q) o += *(const f32x4*)(red + (((q * 4 + slot) * 5 + ct) * 64 + lane) * 4);
            int col = col0 + ct * 16 + 4 * g;
            vals[ct * 4 + 0] = rbf(lo16(xres[ct][0]) + rbf(o[0]));
            vals[ct * 4 + 1] = rbf(hi16(xres[ct][0]) + rbf(o[1]));
            vals[ct * 4 + 2] = rbf(lo16(xres[ct][1]) + rbf(o[2]));
            vals[ct * 4 + 3] = rbf(hi16(xres[ct][1]) + rbf(o[3]));
            u32x2 v;
            v[0] = pack2(vals[ct * 4 + 0], vals[ct * 4 + 1]);
            v[1] = pack2(vals[ct * 4 + 2], vals[ct * 4 + 3]);
            *(u32x2*)(xout + (size_t)row * D_ + col) = v;
            int r = row & 63, cc = col & 63;
            *(u32x2*)(xtile + (size_t)((row >> 6) * 10 + (col >> 6)) * 4096 + r * 64 + (((cc >> 3) ^ (r & 7)) << 3) + (cc & 7)) = v;
        }
        slice_stats(vals, stats + (size_t)row * 8 + cb, g);
    }
}

constexpr int K3_STG = 49152;
constexpr int K3_ST = 3 * K3_STG;
constexpr int K3_SMEM = K3_ST + 2048;

__device__ __forceinline__ float gelu_tanh(float x) {
    float y2 = 1.5957691216057308f * (x + 0.044715f * x * x * x);
    float e = __expf(y2);
    return x - x * __builtin_amdgcn_rcpf(1.0f + e);
}

struct Frag4x4 { bf16x8 a[4]; bf16x4 bl[4], bh[4]; };
#define F4_STEP(C, N, AADDR, SBO)                                                          \
    do {                                                                                   \
        const unsigned a_ = (AADDR), sb_ = (SBO);                                          \
        _Pragma("unroll") for (int ct = 0; ct < 4; ++ct) {                                 \
            LDS_TR(N.bl[ct], blo3[ct] + sb_, 0);  LDS_TR(N.bh[ct], blo3[ct] + sb_, 1024);  \
            if (ct == 0) LDS_B128(N.a[0], a_, 0);                                          \
            if (ct == 1) LDS_B128(N.a[1], a_, 2048);                                       \
            if (ct == 2) LDS_B128(N.a[2], a_, 4096);                                       \
            if (ct == 3) LDS_B128(N.a[3], a_, 6144);                                       \
            SB0();                                                                         \
            bf16x8 b_ = cat8v(C.bl[ct], C.bh[ct]);                                         \
            _Pragma("unroll") for (int rt = 0; rt < 4; ++rt)                               \
                acc[rt][ct] = MFMA(b_, C.a[rt], acc[rt][ct]);                              \
            SB0();                                                                         \
        }                                                                                  \
    } while (0)
#define F4_READ(N, AADDR, SBO)                                                             \
    do {                                                                                   \
        const unsigned a_ = (AADDR), sb_ = (SBO);                                          \
        _Pragma("unroll") for (int ct = 0; ct < 4; ++ct) {                                 \
            LDS_TR(N.bl[ct], blo3[ct] + sb_, 0);  LDS_TR(N.bh[ct], blo3[ct] + sb_, 1024);  \
        }                                                                                  \
        LDS_B128(N.a[0], a_, 0); LDS_B128(N.a[1], a_, 2048); LDS_B128(N.a[2], a_, 4096); LDS_B128(N.a[3], a_, 6144); \
    } while (0)
#define F4_MMA(C)                                                                          \
    do {                                                                                   \
        _Pragma("unroll") for (int ct = 0; ct < 4; ++ct) {                                 \
            bf16x8 b_ = cat8v(C.bl[ct], C.bh[ct]);                                         \
            _Pragma("unroll") for (int rt = 0; rt < 4; ++rt)                               \
                acc[rt][ct] = MFMA(b_, C.a[rt], acc[rt][ct]);                              \
        }                                                                                  \
    } while (0)
#define F4_WAIT(F)                                                                                            \
    asm volatile("s_waitcnt lgkmcnt(0)"                                                                       \
                 : "+v"(F.a[0]), "+v"(F.a[1]), "+v"(F.a[2]), "+v"(F.a[3]), "+v"(F.bl[0]), "+v"(F.bl[1]),       \
                   "+v"(F.bl[2]), "+v"(F.bl[3]), "+v"(F.bh[0]), "+v"(F.bh[1]), "+v"(F.bh[2]), "+v"(F.bh[3]))

__global__ __launch_bounds__(768, 3) void k3_gate_up(const u16* __restrict__ x, const float2* __restrict__ pstats,
                                                     const u16* __restrict__ wg,
                                                     const float* __restrict__ csum, u16* __restrict__ hbuf) {
    extern __shared__ __attribute__((aligned(16))) char smem[];
    const int tid = threadIdx.x, lane = tid & 63, wave = __builtin_amdgcn_readfirstlane(tid >> 6);
    const int g = lane >> 4, l15 = lane & 15;
    const int lin = (blockIdx.x & 7) * 27 + (blockIdx.x >> 3);
    const int rb = lin & 7, cb = lin >> 3;
    const int row0 = rb * 256, col0 = cb * 64;
    float2* stats = (float2*)(smem + K3_ST);
    if (wave >= 8) {
        const int lw = wave - 8;
        const char* srcA = (const char*)x + (size_t)(rb * 4 + lw) * 10 * 8192 + lane * 16;
        const char* srcB = (const char*)wg + (size_t)cb * 10 * 16384 + lw * 4096 + lane * 16;
        auto stage = [&](int s) {
            char* base = smem + (s % 3) * K3_STG;
#pragma unroll
            for (int i = 0; i < 4; ++i) glds16(srcB + (size_t)s * 16384 + i * 1024, base + 32768 + lw * 4096 + i * 1024);
#pragma unroll
            for (int i = 0; i < 8; ++i) glds16(srcA + (size_t)s * 8192 + i * 1024, base + lw * 8192 + i * 1024);
        };
        stage(0); stage(1);
#pragma unroll 1
        for (int b = 0; b < 10; ++b) {
            wait_stages<12, 1>(9 - b);
            BAR_ONLY();
            if (b + 2 < 10) stage(b + 2);
        }
        return;
    }
    if (tid < 256) stats[tid] = combine_stats(pstats + (size_t)(row0 + tid) * 8);
    asm volatile("s_waitcnt lgkmcnt(0)" ::: "memory");
    const int wr = wave >> 1, wc = wave & 1;
    f32x4 acc[4][4];
#pragma unroll
    for (int i = 0; i < 4; ++i)
#pragma unroll
        for (int j = 0; j < 4; ++j) acc[i][j] = (f32x4){0.f, 0.f, 0.f, 0.f};
    const unsigned sm3 = lds_addr(smem);
    unsigned blo3[4];
    {
        int q = l15 >> 2, p4 = lane & 3;
        int sx = q | ((g & 1) << 2);
#pragma unroll
        for (int ct = 0; ct < 4; ++ct) {
            int nt = (ct >> 1) * 4 + wc * 2 + (ct & 1);
            blo3[ct] = sm3 + 32768 + (8 * g + q) * 256 + ((nt ^ sx) * 32) + p4 * 8;
        }
    }
    unsigned aad3[2];
    {
        int r = wr * 64 + l15;
        aad3[0] = sm3 + r * 128 + (((unsigned)(g ^ (r & 7))) << 4);
        aad3[1] = sm3 + r * 128 + (((unsigned)((4 + g) ^ (r & 7))) << 4);
    }
    Frag4x4 fa, fb;
    BAR_ONLY();
    F4_READ(fa, aad3[0], 0);
    F4_WAIT(fa);
#pragma unroll 1
    for (int s = 0; s < 9; ++s) {
        const unsigned so = (s % 3) * K3_STG;
        F4_STEP(fa, fb, aad3[1] + so, so + 8192);
        F4_WAIT(fb);
        BAR_ONLY();
        const unsigned sn = ((s + 1) % 3) * K3_STG;
        F4_STEP(fb, fa, aad3[0] + sn, sn);
        F4_WAIT(fa);
    }
    F4_STEP(fa, fb, aad3[1], 8192);
    F4_WAIT(fb);
    F4_MMA(fb);
    {
        f32x4 cs[4];
#pragma unroll
        for (int c2 = 0; c2 < 2; ++c2) {
            int col = col0 + (wc * 2 + c2) * 16 + 4 * g;
            cs[c2] = *(const f32x4*)(csum + col);
            cs[2 + c2] = *(const f32x4*)(csum + DF_ + col);
        }
#pragma unroll
        for (int rt = 0; rt < 4; ++rt) {
            int rl = wr * 64 + rt * 16 + l15;
            float2 st = stats[rl];
#pragma unroll
            for (int c2 = 0; c2 < 2; ++c2) {
                int col = col0 + (wc * 2 + c2) * 16 + 4 * g;
                float hv[4];
#pragma unroll
                for (int j = 0; j < 4; ++j) {
                    float gg = rbf((acc[rt][c2][j] - st.x * cs[c2][j]) * st.y);
                    float uu = rbf((acc[rt][2 + c2][j] - st.x * cs[2 + c2][j]) * st.y);
                    hv[j] = rbf(gelu_tanh(gg)) * uu;
                }
                u32x2 v;
                v[0] = pack2(hv[0], hv[1]);
                v[1] = pack2(hv[2], hv[3]);
                {
                    int row = row0 + rl, r = row & 63, cc = col & 63;
                    *(u32x2*)(hbuf + (size_t)((row >> 6) * 27 + cb) * 4096 + r * 64 + (((cc >> 3) ^ (r & 7)) << 3) + (cc & 7)) = v;
                }
            }
        }
    }
}

constexpr int K4_STG = 36864;
constexpr int K4_SMEM = 4 * K4_STG;

__global__ __launch_bounds__(768, 3) void k4_down(const u16* __restrict__ hbuf, const u16* __restrict__ wd,
                                                  const u16* __restrict__ xin, u16* __restrict__ xout,
                                                  u16* __restrict__ xtile, float2* __restrict__ stats) {
    extern __shared__ __attribute__((aligned(16))) char smem[];
    const int tid = threadIdx.x, lane = tid & 63, wave = __builtin_amdgcn_readfirstlane(tid >> 6);
    const int g = lane >> 4, l15 = lane & 15;
    const int rb = (blockIdx.x & 7) * 4 + ((blockIdx.x >> 3) & 3), cb = blockIdx.x >> 5;
    const int row0 = rb * 64, col0 = cb * 80;
    constexpr int NS = 14;

    if (wave >= 8) {
        const int lw = wave - 8;
        const char* srcA = (const char*)hbuf + (size_t)rb * 27 * 8192 + lw * 4096 + lane * 16;
        const char* srcB = (const char*)wd + (size_t)cb * 14 * 20480 + lw * 1024 + lane * 16;
        auto stage = [&](int s) {
            char* base = smem + (s & 3) * K4_STG;
            const int sa = (s == NS - 1 && lw >= 2) ? 2 * s - 1 : 2 * s;
#pragma unroll
            for (int i = 0; i < 4; ++i) glds16(srcA + (size_t)sa * 8192 + i * 1024, base + lw * 4096 + i * 1024);
#pragma unroll
            for (int i = 0; i < 5; ++i) glds16(srcB + (size_t)s * 20480 + i * 4096, base + 16384 + lw * 1024 + i * 4096);
        };
        stage(0); stage(1); stage(2);
#pragma unroll 1
        for (int b = 0; b < NS; ++b) {
            wait_stages<9, 2>(NS - 1 - b);
            BAR_ONLY();
            if (b + 3 < NS) stage(b + 3);
        }
        return;
    }

    u32x2 xres[5];
    if (wave < 4) {
#pragma unroll
        for (int ct = 0; ct < 5; ++ct) xres[ct] = *(const u32x2*)(xin + (size_t)(row0 + wave * 16 + l15) * D_ + col0 + ct * 16 + 4 * g);
    }
    const int wr = wave & 1, kq = wave >> 1;
    f32x4 acc[2][5];
#pragma unroll
    for (int i = 0; i < 2; ++i)
#pragma unroll
        for (int j = 0; j < 5; ++j) acc[i][j] = (f32x4){0.f, 0.f, 0.f, 0.f};
    {
        const unsigned sm = lds_addr(smem);
        unsigned blo, bhi;
        wfrag_addr<160>(sm + 16384 + kq * 5120, lane, blo, bhi);
        const int r = wr * 32 + l15;
        const unsigned aad = sm + (kq >> 1) * 8192 + r * 128 + (((unsigned)(((kq & 1) * 4 + g) ^ (r & 7))) << 4);
        Frag2x5 fa, fb;
        BAR_ONLY();
        G5_READ(fa, aad, 2048, blo, bhi);
        G5_WAIT(fa);
#pragma unroll 1
        for (int s = 0; s < NS - 2; s += 2) {
            BAR_ONLY();
            { const unsigned so = ((s + 1) & 3) * K4_STG; G5_STEP(fa, fb, aad + so, 2048, blo + so, bhi + so); }
            G5_WAIT(fb);
            BAR_ONLY();
            { const unsigned so = ((s + 2) & 3) * K4_STG; G5_STEP(fb, fa, aad + so, 2048, blo + so, bhi + so); }
            G5_WAIT(fa);
        }
        BAR_ONLY();
        { const unsigned so = ((NS - 1) & 3) * K4_STG; G5_STEP(fa, fb, aad + so, 2048, blo + so, bhi + so); }
        G5_WAIT(fb);
        if (kq < 2) G5_MMA(fb);
    }
    __syncthreads();
    float* red = (float*)smem;
#pragma unroll
    for (int rt = 0; rt < 2; ++rt)
#pragma unroll
        for (int ct = 0; ct < 5; ++ct) *(f32x4*)(red + (((kq * 4 + wr * 2 + rt) * 5 + ct) * 64 + lane) * 4) = acc[rt][ct];
    __syncthreads();
    if (wave < 4) {
        const int row = row0 + wave * 16 + l15;
        float vals[20];
#pragma unroll
        for (int ct = 0; ct < 5; ++ct) {
            f32x4 o = *(const f32x4*)(red + ((wave * 5 + ct) * 64 + lane) * 4);
#pragma unroll
            for (int q = 1; q < 4; ++q) o += *(const f32x4*)(red + (((q * 4 + wave) * 5 + ct) * 64 + lane) * 4);
            int col = col0 + ct * 16 + 4 * g;
            vals[ct * 4 + 0] = rbf(lo16(xres[ct][0]) + rbf(o[0]));
            vals[ct * 4 + 1] = rbf(hi16(xres[ct][0]) + rbf(o[1]));
            vals[ct * 4 + 2] = rbf(lo16(xres[ct][1]) + rbf(o[2]));
            vals[ct * 4 + 3] = rbf(hi16(xres[ct][1]) + rbf(o[3]));
            u32x2 v;
            v[0] = pack2(vals[ct * 4 + 0], vals[ct * 4 + 1]);
            v[1] = pack2(vals[ct * 4 + 2], vals[ct * 4 + 3]);
            *(u32x2*)(xout + (size_t)row * D_ + col) = v;
            if (xtile) {
                int r = row & 63, cc = col & 63;
                *(u32x2*)(xtile + (size_t)(rb * 10 + (col >> 6)) * 4096 + r * 64 + (((cc >> 3) ^ (r & 7)) << 3) + (cc & 7)) = v;
            }
        }
        if (xtile) slice_stats(vals, stats + (size_t)row * 8 + cb, g);
    }
}

extern "C" void kernel_launch(void* const* d_in, const int* in_sizes, int n_in,
                              void* d_out, int out_size, void* d_ws, size_t ws_size,
                              hipStream_t stream) {
    const u16* A_x = (const u16*)d_in[0];
    const u16* wqkv = (const u16*)d_in[1];
    const u16* wphi = (const u16*)d_in[2];
    const u16* wo = (const u16*)d_in[3];
    const u16* wg = (const u16*)d_in[5];
    const u16* wu = (const u16*)d_in[6];
    const u16* wd = (const u16*)d_in[7];
    u16* out = (u16*)d_out;

    char* ws = (char*)d_ws;
    u16* x1 = (u16*)(ws);
    u16* x2 = (u16*)(ws + (4u << 20));
    char* phiq = ws + (8u << 20);
    float* kvp = (float*)(ws + (16u << 20));
    u16* kvt = (u16*)(ws + (24u << 20));
    float2* stats = (float2*)(ws + (25u << 20));
    float* csum = (float*)(ws + (26u << 20));
    float2* stats0 = (float2*)(ws + (27u << 20));
    u16* hbuf = (u16*)(ws + (28u << 20));
    u16* x1t = (u16*)(ws + (36u << 20));
    u16* x0t = (u16*)(ws + (52u << 20));
    char* wt = ws + (40u << 20);
    const u16* wq_t = (const u16*)wt;
    const u16* wo_t = (const u16*)(wt + (size_t)WQ_CH * 16);
    const u16* wgu_t = (const u16*)(wt + (size_t)(WQ_CH + WO_CH) * 16);
    const u16* wd_t = (const u16*)(wt + (size_t)(WQ_CH + WO_CH + WGU_CH) * 16);

    (void)hipFuncSetAttribute((const void*)k1_qkv, hipFuncAttributeMaxDynamicSharedMemorySize, K1_SMEM);
    (void)hipFuncSetAttribute((const void*)k2_attn_out, hipFuncAttributeMaxDynamicSharedMemorySize, K2_SMEM);
    (void)hipFuncSetAttribute((const void*)k3_gate_up, hipFuncAttributeMaxDynamicSharedMemorySize, K3_SMEM);
    (void)hipFuncSetAttribute((const void*)k4_down, hipFuncAttributeMaxDynamicSharedMemorySize, K4_SMEM);

    k0_prep<<<K0_XBLK + K0_CSBLK + K0_CPBLK, 256, 0, stream>>>(A_x, wqkv, wo, wg, wu, wd, (u32x4*)wt, csum, x0t, stats0);
    const u16* xin = A_x;
    for (int layer = 0; layer < 2; ++layer) {
        u16* xmid = x1;
        u16* xo = (layer == 0) ? x2 : out;
        k1_qkv<<<256, 768, K1_SMEM, stream>>>(x0t, stats0, wq_t, csum, wphi, phiq, kvp);
        kr_reduce<<<320, 256, 0, stream>>>(kvp, kvt);
        k2_attn_out<<<256, 768, K2_SMEM, stream>>>(xin, phiq, kvt, wo_t, xmid, x1t, stats);
        k3_gate_up<<<216, 768, K3_SMEM, stream>>>(x1t, stats, wgu_t, csum, hbuf);
        k4_down<<<256, 768, K4_SMEM, stream>>>(hbuf, wd_t, xmid, xo, layer == 0 ? x0t : (u16*)nullptr, stats0);
        xin = x2;
    }
}
```
